```python
import torch

def _softmax(x, axis=-1):
    return torch.softmax(x, dim=axis)
def _gelu(x):
    return torch.nn.functional.gelu(x, approximate="tanh")
def _silu(x):
    return torch.nn.functional.silu(x)
def _sigmoid(x):
    return torch.sigmoid(x)
def _layernorm(x, axis=-1, eps=1e-5):
    m = x.mean(dim=axis, keepdim=True)
    v = x.var(dim=axis, keepdim=True, unbiased=False)
    return (x - m) / torch.sqrt(v + eps)
def _scatter_add(tgt, idx, src, axis=0):
    return tgt.clone().index_add_(axis, idx, src)
def _segment_sum(src, seg, n):
    out = torch.zeros((n,) + src.shape[1:], dtype=src.dtype)
    return out.index_add_(0, seg, src)
def _pad(x, pw):
    flat = tuple(v for pair in reversed(pw) for v in pair)
    return torch.nn.functional.pad(x, flat)

kernel_name = 'gated_mix__sliding_block_cached__dense_activation_silu__decode'

def setup_inputs():

    _w = (lambda a: (lambda t: t.to(torch.bfloat16) if t.is_floating_point() else t)(torch.as_tensor(a)))
    def _A_setup():
        B, Sq, D = 8, 1, 448
        Hq, Hkv, Sk, Dh = 16, 8, 8192, 256
        assert Hq % Hkv == 0, (Hq, Hkv)
        return {
            "x": _w((lambda *s: torch.randn(*s, dtype=torch.bfloat16) * 0.1)(B, Sq, D)),
            "k_cache": _w((lambda *s: torch.randn(*s, dtype=torch.bfloat16) * 3.2)(B, Hkv, Sk, Dh)),
            "v_cache": _w((lambda *s: torch.randn(*s, dtype=torch.bfloat16) * 3.2)(B, Hkv, Sk, Dh)),
            "wq": _w((lambda *s: torch.randn(*s, dtype=torch.bfloat16) * 3.2)(D, Hq * Dh) * (D ** -0.5)),
            "wo": _w((lambda *s: torch.randn(*s, dtype=torch.bfloat16) * 3.2)(Hq * Dh, D) * ((Hq * Dh) ** -0.5)),
        }
    _A_in = _A_setup()

    def _B_setup():
        B, S, D, F = 8, 1, 448, 1792
        return {
            "x": _w((lambda *s: torch.randn(*s, dtype=torch.bfloat16) * 0.1)(B, S, D)),
            "w1": _w((lambda *s: torch.randn(*s, dtype=torch.bfloat16) * 3.2)(D, F) * (D ** -0.5)),
            "w2": _w((lambda *s: torch.randn(*s, dtype=torch.bfloat16) * 3.2)(F, D) * (F ** -0.5)),
        }
    _B_in = _B_setup()

    return {
        "A_x": _A_in["x"],
        "A_k_cache": _A_in["k_cache"],
        "A_v_cache": _A_in["v_cache"],
        "A_wq": _A_in["wq"],
        "A_wo": _A_in["wo"],
        "B_x": _B_in["x"],
        "B_w1": _B_in["w1"],
        "B_w2": _B_in["w2"],
        "gate_w": _w((lambda *s: torch.randn(*s, dtype=torch.bfloat16) * 0.1)(448) * 16.0),
    }

def reference(A_x, A_k_cache, A_v_cache, A_wq, A_wo, B_x, B_w1, B_w2, gate_w):
    def _A_ref(x, k_cache, v_cache, wq, wo):
        B, Sq, D = x.shape
        Hq, Dh = 16, 256
        Hkv, Sk = k_cache.shape[1], k_cache.shape[2]
        W = min(4096, max(256, Sk // 4))
        rep = Hq // Hkv
        q = (lambda x, *d: x.permute(*d))((x @ wq).reshape(B, Sq, Hq, Dh), 0, 2, 1, 3)
        k = (lambda x, n, axis: torch.repeat_interleave(x, n, dim=axis))(k_cache[:, :, Sk - W:, :], rep, axis=1)
        v = (lambda x, n, axis: torch.repeat_interleave(x, n, dim=axis))(v_cache[:, :, Sk - W:, :], rep, axis=1)
        scores = torch.einsum("bhqd,bhkd->bhqk", q, k) * (Dh ** -0.5)
        p = _softmax(scores, axis=-1)
        o = torch.einsum("bhqk,bhkd->bhqd", p, v)
        return (lambda x, *d: x.permute(*d))(o, 0, 2, 1, 3).reshape(B, Sq, Hq * Dh) @ wo

    def _B_ref(x, w1, w2):
        h = torch.einsum("bsd,df->bsf", x, w1)
        h = _silu(h)
        out = torch.einsum("bsf,fd->bsd", h, w2)
        return out

    x = A_x
    g = _sigmoid((x * gate_w).sum(-1, keepdims=True))
    a = _A_ref(x, A_k_cache, A_v_cache, A_wq, A_wo)
    b = _B_ref(x, B_w1, B_w2)
    return g * a + (1 - g) * b
```

```cpp
#include <hip/hip_runtime.h>
#include <hip/hip_bf16.h>
#include <stdint.h>

typedef unsigned u32x4 __attribute__((ext_vector_type(4)));
typedef float f32x4 __attribute__((ext_vector_type(4)));
typedef __bf16 bf16x2 __attribute__((ext_vector_type(2)));

constexpr int B = 8, D = 448, HQ = 16, HKV = 8, SK = 8192, DH = 256, W = 2048, F = 1792;
constexpr int NSPLIT = 8;
constexpr int KPS = W / NSPLIT;

__device__ __forceinline__ float bflo(unsigned u) { return __builtin_bit_cast(float, u << 16); }
__device__ __forceinline__ float bfhi(unsigned u) { return __builtin_bit_cast(float, u & 0xffff0000u); }
__device__ __forceinline__ float bf2f(unsigned short u) { return __builtin_bit_cast(float, (unsigned)u << 16); }
__device__ __forceinline__ unsigned short f2bf(float f) { __bf16 h = (__bf16)f; return __builtin_bit_cast(unsigned short, h); }
__device__ __forceinline__ float rbf(float f) { return bf2f(f2bf(f)); }
__device__ __forceinline__ float dot2(unsigned k, unsigned q, float acc) {
  return __builtin_amdgcn_fdot2_f32_bf16(__builtin_bit_cast(bf16x2, k), __builtin_bit_cast(bf16x2, q), acc, false);
}

__global__ void __launch_bounds__(256) k_pre(const unsigned short* x, const unsigned short* wq, const unsigned short* w1,
                                             unsigned short* q, unsigned short* h) {
  __shared__ float xs[B * D];
  __shared__ float red[32 * 8 * 65];
  const int tid = threadIdx.x;
  for (int i = tid; i < B * D; i += 256) xs[i] = bf2f(x[i]);
  __syncthreads();
  const bool isq = blockIdx.x < 64;
  const int cb = isq ? blockIdx.x : blockIdx.x - 64;
  const unsigned short* w = isq ? wq : w1;
  const int ld = isq ? HQ * DH : F;
  const int r = tid >> 3, c = tid & 7;
  float acc[8][8];
#pragma unroll
  for (int b = 0; b < 8; ++b)
#pragma unroll
    for (int j = 0; j < 8; ++j) acc[b][j] = 0.f;
#pragma unroll
  for (int i = 0; i < 14; ++i) {
    const int row = r + 32 * i;
    const u32x4 wv = *(const u32x4*)(w + (size_t)row * ld + cb * 64 + c * 8);
    float wf[8];
#pragma unroll
    for (int k = 0; k < 4; ++k) { wf[2 * k] = bflo(wv[k]); wf[2 * k + 1] = bfhi(wv[k]); }
#pragma unroll
    for (int b = 0; b < 8; ++b) { const float xv = xs[b * D + row];
#pragma unroll
      for (int j = 0; j < 8; ++j) acc[b][j] = fmaf(xv, wf[j], acc[b][j]); }
  }
#pragma unroll
  for (int b = 0; b < 8; ++b)
#pragma unroll
    for (int j = 0; j < 8; ++j) red[(r * 8 + b) * 65 + c * 8 + j] = acc[b][j];
  __syncthreads();
  for (int o = tid; o < 512; o += 256) {
    const int b = o >> 6, col = o & 63;
    float s = 0.f;
    for (int rr = 0; rr < 32; ++rr) s += red[(rr * 8 + b) * 65 + col];
    if (isq) q[b * (HQ * DH) + cb * 64 + col] = f2bf(s);
    else { const float hv = rbf(s); h[b * F + cb * 64 + col] = f2bf(hv / (1.f + __expf(-hv))); }
  }
}

template <int CTRL> __device__ __forceinline__ float dpp(float x) {
  return __builtin_bit_cast(float, __builtin_amdgcn_mov_dpp(__builtin_bit_cast(int, x), CTRL, 0xf, 0xf, true));
}
constexpr int XOR1 = 0xB1, XOR2 = 0x4E, XOR7 = 0x141, XOR8 = 0x128;

__global__ void __launch_bounds__(256) k_attn(const unsigned short* kc, const unsigned short* vc, const unsigned short* q,
                                              float* po, float* pml) {
  __shared__ float sc[2][KPS];
  __shared__ float oacc[4][2][DH];
  __shared__ float wm[2][4];
  const int tid = threadIdx.x, lane = tid & 63, wave = tid >> 6, r = lane >> 4, c = lane & 15;
  const int bh = blockIdx.x / NSPLIT, s = blockIdx.x % NSPLIT;
  const int b = bh >> 3, hk = bh & 7;
  const size_t base = ((size_t)bh * SK + (SK - W) + s * KPS + wave * (KPS / 4)) * DH;
  u32x4 qv[2][2];
#pragma unroll
  for (int g = 0; g < 2; ++g)
#pragma unroll
    for (int hf = 0; hf < 2; ++hf) qv[g][hf] = *(const u32x4*)(q + b * 4096 + (hk * 2 + g) * 256 + hf * 128 + c * 8);
#pragma unroll
  for (int t = 0; t < KPS / 4 / 32; ++t) {
    u32x4 kv[8][2];
#pragma unroll
    for (int i = 0; i < 8; ++i)
#pragma unroll
      for (int hf = 0; hf < 2; ++hf)
        kv[i][hf] = __builtin_nontemporal_load((const u32x4*)(kc + base + (size_t)(t * 32 + 4 * i + r) * DH + hf * 128 + c * 8));
    float x[16];
#pragma unroll
    for (int i = 0; i < 8; ++i)
#pragma unroll
      for (int g = 0; g < 2; ++g) { float d = 0.f;
#pragma unroll
        for (int hf = 0; hf < 2; ++hf)
#pragma unroll
          for (int k = 0; k < 4; ++k) d = dot2(kv[i][hf][k], qv[g][hf][k], d);
        x[i * 2 + g] = d; }
#define TR_STEP(HALF, CTRL, BIT) _Pragma("unroll") for (int hh = 0; hh < (HALF); ++hh) { \
      const float lo_ = x[hh], hi_ = x[hh + (HALF)]; const float t1_ = lo_ + dpp<CTRL>(lo_), t2_ = hi_ + dpp<CTRL>(hi_); \
      x[hh] = (c & (BIT)) ? t2_ : t1_; }
    TR_STEP(8, XOR8, 8) TR_STEP(4, XOR7, 4) TR_STEP(2, XOR2, 2) TR_STEP(1, XOR1, 1)
#undef TR_STEP
    sc[c & 1][wave * (KPS / 4) + t * 32 + 4 * (c >> 1) + r] = x[0] * 0.0625f;
  }
  __syncthreads();
  float p[2], mx[2];
#pragma unroll
  for (int g = 0; g < 2; ++g) { float v = sc[g][tid];
    float m = v;
    for (int o = 32; o >= 1; o >>= 1) m = fmaxf(m, __shfl_xor(m, o));
    if (lane == 0) wm[g][wave] = m;
    p[g] = v; }
  __syncthreads();
  float ls[2];
#pragma unroll
  for (int g = 0; g < 2; ++g) { mx[g] = fmaxf(fmaxf(wm[g][0], wm[g][1]), fmaxf(wm[g][2], wm[g][3]));
    p[g] = __expf(p[g] - mx[g]); sc[g][tid] = p[g];
    float l = p[g]; for (int o = 32; o >= 1; o >>= 1) l += __shfl_xor(l, o); ls[g] = l; }
  __syncthreads();
  if (lane == 0) { wm[0][wave] = ls[0]; wm[1][wave] = ls[1]; }
  float acc[2][2][8];
#pragma unroll
  for (int g = 0; g < 2; ++g)
#pragma unroll
    for (int hf = 0; hf < 2; ++hf)
#pragma unroll
      for (int j = 0; j < 8; ++j) acc[g][hf][j] = 0.f;
#pragma unroll
  for (int t = 0; t < KPS / 4 / 32; ++t) {
    u32x4 vv[8][2];
#pragma unroll
    for (int i = 0; i < 8; ++i)
#pragma unroll
      for (int hf = 0; hf < 2; ++hf)
        vv[i][hf] = __builtin_nontemporal_load((const u32x4*)(vc + base + (size_t)(t * 32 + 4 * i + r) * DH + hf * 128 + c * 8));
#pragma unroll
    for (int i = 0; i < 8; ++i) {
      const int key = wave * (KPS / 4) + t * 32 + 4 * i + r;
      const float p0 = sc[0][key], p1 = sc[1][key];
#pragma unroll
      for (int hf = 0; hf < 2; ++hf)
#pragma unroll
        for (int k = 0; k < 4; ++k) { const float lo = bflo(vv[i][hf][k]), hi = bfhi(vv[i][hf][k]);
          acc[0][hf][2 * k] = fmaf(p0, lo, acc[0][hf][2 * k]); acc[0][hf][2 * k + 1] = fmaf(p0, hi, acc[0][hf][2 * k + 1]);
          acc[1][hf][2 * k] = fmaf(p1, lo, acc[1][hf][2 * k]); acc[1][hf][2 * k + 1] = fmaf(p1, hi, acc[1][hf][2 * k + 1]); }
    }
  }
#pragma unroll
  for (int g = 0; g < 2; ++g)
#pragma unroll
    for (int hf = 0; hf < 2; ++hf)
#pragma unroll
      for (int j = 0; j < 8; ++j) { float v = acc[g][hf][j]; v += __shfl_xor(v, 16); v += __shfl_xor(v, 32);
        if (r == 0) oacc[wave][g][hf * 128 + c * 8 + j] = v; }
  __syncthreads();
#pragma unroll
  for (int g = 0; g < 2; ++g) {
    const float v = oacc[0][g][tid] + oacc[1][g][tid] + oacc[2][g][tid] + oacc[3][g][tid];
    const int row = (b * HQ + hk * 2 + g) * NSPLIT + s;
    po[(size_t)row * DH + tid] = v;
    if (tid == 0) { pml[row * 2] = mx[g]; pml[row * 2 + 1] = wm[g][0] + wm[g][1] + wm[g][2] + wm[g][3]; }
  }
}

__global__ void __launch_bounds__(256) k_proj(const float* po, const float* pml, const unsigned short* h, const unsigned short* wo,
                                              const unsigned short* w2, float* slab) {
  __shared__ float act[8][64];
  __shared__ float red[4][8][448];
  const int tid = threadIdx.x, blk = blockIdx.x;
  const bool isa = blk < 64;
  for (int o = tid; o < 512; o += 256) {
    const int b = o >> 6, e = o & 63;
    if (isa) {
      const int hd = blk >> 2, d = (blk & 3) * 64 + e;
      const int row = (b * HQ + hd) * NSPLIT;
      float M = -INFINITY;
      for (int s = 0; s < NSPLIT; ++s) M = fmaxf(M, pml[(row + s) * 2]);
      float L = 0.f, acc = 0.f;
      for (int s = 0; s < NSPLIT; ++s) { const float w = __expf(pml[(row + s) * 2] - M); L = fmaf(w, pml[(row + s) * 2 + 1], L); acc = fmaf(w, po[(size_t)(row + s) * DH + d], acc); }
      act[b][e] = rbf(acc / L);
    } else act[b][e] = bf2f(h[b * F + (blk - 64) * 64 + e]);
  }
  __syncthreads();
  const unsigned short* w = isa ? wo + (size_t)blk * 64 * D : w2 + (size_t)(blk - 64) * 64 * D;
  const int wave = tid >> 6, lane = tid & 63;
  float acc[8][8];
#pragma unroll
  for (int b = 0; b < 8; ++b)
#pragma unroll
    for (int j = 0; j < 8; ++j) acc[b][j] = 0.f;
  if (lane < 56) {
#pragma unroll
    for (int i = 0; i < 16; ++i) {
      const int row = wave * 16 + i;
      const u32x4 wv = *(const u32x4*)(w + (size_t)row * D + lane * 8);
      float wf[8];
#pragma unroll
      for (int k = 0; k < 4; ++k) { wf[2 * k] = bflo(wv[k]); wf[2 * k + 1] = bfhi(wv[k]); }
#pragma unroll
      for (int b = 0; b < 8; ++b) { const float a = act[b][row];
#pragma unroll
        for (int j = 0; j < 8; ++j) acc[b][j] = fmaf(a, wf[j], acc[b][j]); }
    }
#pragma unroll
    for (int b = 0; b < 8; ++b)
#pragma unroll
      for (int j = 0; j < 8; ++j) red[wave][b][lane * 8 + j] = acc[b][j];
  }
  __syncthreads();
  for (int o = tid; o < 8 * 448; o += 256) {
    const int b = o / 448, col = o % 448;
    slab[(size_t)blk * 3584 + o] = red[0][b][col] + red[1][b][col] + red[2][b][col] + red[3][b][col];
  }
}

__global__ void __launch_bounds__(256) k_final(const float* slab, const unsigned short* x, const unsigned short* gw, unsigned short* out) {
  const int o = blockIdx.x * 256 + threadIdx.x;
  const int b = o / 448;
  float a = 0.f, bb = 0.f;
  for (int i = 0; i < 64; ++i) a += slab[(size_t)i * 3584 + o];
  for (int i = 64; i < 92; ++i) bb += slab[(size_t)i * 3584 + o];
  float gs = 0.f;
  for (int d = 0; d < D; ++d) gs += rbf(bf2f(x[b * D + d]) * bf2f(gw[d]));
  gs = rbf(gs);
  const float g = rbf(1.f / (1.f + __expf(-gs)));
  a = rbf(a); bb = rbf(bb);
  out[o] = f2bf(rbf(g * a) + rbf(rbf(1.f - g) * bb));
}

extern "C" void kernel_launch(void* const* d_in, const int* in_sizes, int n_in, void* d_out, int out_size, void* d_ws, size_t ws_size,
                              hipStream_t stream) {
  const unsigned short* x = (const unsigned short*)d_in[0];
  const unsigned short* kc = (const unsigned short*)d_in[1];
  const unsigned short* vc = (const unsigned short*)d_in[2];
  const unsigned short* wq = (const unsigned short*)d_in[3];
  const unsigned short* wo = (const unsigned short*)d_in[4];
  const unsigned short* w1 = (const unsigned short*)d_in[6];
  const unsigned short* w2 = (const unsigned short*)d_in[7];
  const unsigned short* gw = (const unsigned short*)d_in[8];
  char* ws = (char*)d_ws;
  unsigned short* q = (unsigned short*)(ws);
  unsigned short* h = (unsigned short*)(ws + 65536);
  float* pml = (float*)(ws + 131072);
  float* po = (float*)(ws + 262144);
  float* slab = (float*)(ws + 262144 + 4 * 1048576);
  k_pre<<<92, 256, 0, stream>>>(x, wq, w1, q, h);
  k_attn<<<64 * NSPLIT, 256, 0, stream>>>(kc, vc, q, po, pml);
  k_proj<<<92, 256, 0, stream>>>(po, pml, h, wo, w2, slab);
  k_final<<<14, 256, 0, stream>>>(slab, x, gw, (unsigned short*)d_out);
}
```

```cpp
#include <hip/hip_runtime.h>
#include <hip/hip_bf16.h>
#include <stdint.h>

#define LAS __attribute__((address_space(3)))
typedef unsigned u32x4 __attribute__((ext_vector_type(4)));
typedef unsigned u32x2 __attribute__((ext_vector_type(2)));
typedef float f32x4 __attribute__((ext_vector_type(4)));
typedef float f32x2 __attribute__((ext_vector_type(2)));
typedef __bf16 bf16x8 __attribute__((ext_vector_type(8)));
typedef __bf16 bf16x2 __attribute__((ext_vector_type(2)));
typedef short s16x4 __attribute__((ext_vector_type(4)));
typedef unsigned long long u64;

#define INFL 8

constexpr int SK = 8192, WIN = 2048;
constexpr unsigned WS_E = 0, WS_FLAG = 4096, WS_ST = 8192, WS_H = 32768, WS_Q = 65536, WS_O = WS_Q + 524288, WS_P = WS_O + 262144,
                   WS_END = WS_P + 224 * 2048;
constexpr unsigned OOB = 0x7ffffff0u;
constexpr unsigned SENT = 0xffffffffu;
constexpr unsigned L_A = 131072, L_X = L_A + 14336, L_G = L_X + 7168, L_C = L_G + 1024, L_Q = L_C + 8192, L_F = L_Q + 1024, L_END = L_F + 256;
constexpr unsigned F_Q = L_F, F_ST = L_F + 4, F_F = L_F + 8, F_ARR = L_F + 12, L_STL = L_F + 64, L_FF = L_F + 128;
constexpr int SPIN_LDS = 1 << 20, SPIN_MEM = 1 << 16;
constexpr float SCL = 0.0625f * 1.4426950408889634f;

__device__ __forceinline__ u32x4 make_srd(const void* p, unsigned bytes) {
  const u64 a = (u64)p;
  u32x4 r; r[0] = (unsigned)a; r[1] = (unsigned)(a >> 32) & 0xffffu; r[2] = bytes; r[3] = 0x00020000u; return r;
}
__device__ __forceinline__ void dma1(unsigned ldsaddr, unsigned voff, u32x4 srd, unsigned soff) {
  asm volatile("s_mov_b32 m0, %0\n\ts_nop 0\n\tbuffer_load_dwordx4 %1, %2, %3 offen nt lds" ::"s"(ldsaddr), "v"(voff), "s"(srd), "s"(soff) : "memory");
}
#define WPOL "nt "
#define WINFL 15
__device__ __forceinline__ void dma1w(unsigned ldsaddr, unsigned voff, u32x4 srd, unsigned soff) {
  asm volatile("s_mov_b32 m0, %0\n\ts_waitcnt vmcnt(%4)\n\tbuffer_load_dwordx4 %1, %2, %3 offen " WPOL "lds" ::"s"(ldsaddr), "v"(voff), "s"(srd), "s"(soff), "i"(WINFL) : "memory");
}
__device__ __forceinline__ void dma4(unsigned ldsaddr, unsigned v0, unsigned v1, unsigned v2, unsigned v3, u32x4 srd, unsigned soff) {
  asm volatile("s_mov_b32 m0, %0\n\t"
               "s_waitcnt vmcnt(%7) lgkmcnt(0)\n\tbuffer_load_dwordx4 %1, %5, %6 offen nt lds\n\t"
               "s_waitcnt vmcnt(%7)\n\tbuffer_load_dwordx4 %2, %5, %6 offen offset:1024 nt lds\n\t"
               "s_waitcnt vmcnt(%7)\n\tbuffer_load_dwordx4 %3, %5, %6 offen offset:2048 nt lds\n\t"
               "s_waitcnt vmcnt(%7)\n\tbuffer_load_dwordx4 %4, %5, %6 offen offset:3072 nt lds"
               ::"s"(ldsaddr), "v"(v0), "v"(v1), "v"(v2), "v"(v3), "s"(srd), "s"(soff), "i"(INFL - 1) : "memory");
}
#define WAITVM0() asm volatile("s_waitcnt vmcnt(0)" ::: "memory")
#define WAITLGKM0() asm volatile("s_waitcnt lgkmcnt(0)" ::: "memory")
#define CFENCE() asm volatile("" ::: "memory")

__device__ __forceinline__ f32x4 mfma(u32x4 a, u32x4 b, f32x4 c) {
  return __builtin_amdgcn_mfma_f32_16x16x32_bf16(__builtin_bit_cast(bf16x8, a), __builtin_bit_cast(bf16x8, b), c, 0, 0, 0);
}
__device__ __forceinline__ f32x4 mfma16(u32x2 a, u32x2 b, f32x4 c) {
  return __builtin_amdgcn_mfma_f32_16x16x16bf16_1k(__builtin_bit_cast(s16x4, a), __builtin_bit_cast(s16x4, b), c, 0, 0, 0);
}
__device__ __forceinline__ u32x2 trread(LAS unsigned char* p) {
  return __builtin_bit_cast(u32x2, __builtin_amdgcn_ds_read_tr16_b64_v4i16((LAS s16x4*)p));
}
__device__ __forceinline__ u32x4 trfrag(LAS unsigned char* p0, LAS unsigned char* p1) {
  const u32x2 a = trread(p0), b = trread(p1); return u32x4{a[0], a[1], b[0], b[1]};
}
__device__ __forceinline__ unsigned pk(float lo, float hi) {
  const bf16x2 v = {(__bf16)lo, (__bf16)hi}; return __builtin_bit_cast(unsigned, v);
}
__device__ __forceinline__ float rbf(float f) { return (float)(__bf16)f; }
__device__ __forceinline__ float asf(unsigned u) { return __builtin_bit_cast(float, u); }
__device__ __forceinline__ unsigned asu(float f) { return __builtin_bit_cast(unsigned, f); }
__device__ __forceinline__ unsigned nosent(unsigned u) { return u < SENT - 1u ? u : SENT - 1u; }
__device__ __forceinline__ unsigned umax(unsigned a, unsigned b) { return a > b ? a : b; }
__device__ __forceinline__ unsigned umax4(u32x4 v) { return umax(umax(v[0], v[1]), umax(v[2], v[3])); }
template <int CTRL> __device__ __forceinline__ float dpp(float x) {
  return __builtin_bit_cast(float, __builtin_amdgcn_mov_dpp(__builtin_bit_cast(int, x), CTRL, 0xf, 0xf, true));
}
constexpr int D_X1 = 0xB1, D_X2 = 0x4E, D_R4 = 0x124, D_R8 = 0x128;
__device__ __forceinline__ float xrow_max(float x) {
  auto s = __builtin_amdgcn_permlane16_swap(asu(x), asu(x), false, false); x = fmaxf(asf(s[0]), asf(s[1]));
  auto t = __builtin_amdgcn_permlane32_swap(asu(x), asu(x), false, false); return fmaxf(asf(t[0]), asf(t[1]));
}
__device__ __forceinline__ float xrow_sum(float x) {
  auto s = __builtin_amdgcn_permlane16_swap(asu(x), asu(x), false, false); x = asf(s[0]) + asf(s[1]);
  auto t = __builtin_amdgcn_permlane32_swap(asu(x), asu(x), false, false); return asf(t[0]) + asf(t[1]);
}
__device__ __forceinline__ void lds_wait_ge(LAS unsigned char* L, unsigned off, unsigned want) {
#pragma nounroll
  for (int i = 0; i < SPIN_LDS; ++i) {
    if ((unsigned)__builtin_amdgcn_readfirstlane(*(volatile LAS unsigned*)(L + off)) >= want) break;
    __builtin_amdgcn_s_sleep(1);
  }
  CFENCE();
}
#define LD128(off) __builtin_amdgcn_raw_buffer_load_b128(rws, (off), 0, 16)
#define ST128(v, off) __builtin_amdgcn_raw_buffer_store_b128((v), rws, (off), 0, 16)
#define ST64(v, off) __builtin_amdgcn_raw_buffer_store_b64((v), rws, (off), 0, 16)

#define STAMP(i) do { } while (0)
#define STAMP_OUT() do { } while (0)
#define STAMP_DECL() do { } while (0)

__global__ void __launch_bounds__(320, 2)
gated_mix_kernel(const unsigned short* x, const unsigned short* kc, const unsigned short* vc,
                 const unsigned short* wq, const unsigned short* wo, const unsigned short* w1,
                 const unsigned short* w2, const unsigned short* gw, unsigned short* out,
                 unsigned char* ws) {
  __shared__ __attribute__((aligned(16))) unsigned char smem[L_END];
  LAS unsigned char* L = (LAS unsigned char*)smem;
  const unsigned lds0 = (unsigned)(size_t)L;
  asm volatile("" ::"s"(x), "s"(kc), "s"(vc), "s"(wq), "s"(gw), "s"(ws));
  const int tid = threadIdx.x, lane = tid & 63, wave = __builtin_amdgcn_readfirstlane(tid >> 6);
  const int wg = blockIdx.x, hk = wg & 7, r = wg >> 3, b = r >> 2, s = r & 3;
  const int g = lane >> 4, n = lane & 15;
  const __amdgpu_buffer_rsrc_t rws = __builtin_amdgcn_make_buffer_rsrc((void*)ws, 0, (int)WS_END, 0x00020000);

  STAMP_DECL();
  if (wave < 4) {
    const size_t wb = ((size_t)(b * 8 + hk) * SK + (SK - WIN)) * 256;
    const u32x4 rk = make_srd(kc + wb, WIN * 512), rv = make_srd(vc + wb, WIN * 512);
    const unsigned hi = lane >> 5, vb = hi * 512 + (((lane & 31) ^ (hi << 1)) << 4);
    const unsigned v0 = vb, v1 = vb ^ 64, v2 = vb ^ 128, v3 = vb ^ 192;
    const unsigned ring = wave * 32768, so0 = (s * 4 + wave) * 8192;
    auto issue = [&](int t) {
      const u32x4 rs = t < 8 ? rk : rv;
      const unsigned so = so0 + (t & 7) * 131072, m = lds0 + ring + (t & 3) * 8192;
      dma4(m, v0, v1, v2, v3, rs, so);
      dma4(m + 4096, v0, v1, v2, v3, rs, so + 4096);
    };
    STAMP(0);
#pragma nounroll
    for (int t = 0; t < 8; ++t) {
      dma4(lds0 + ring + t * 4096, v0, v1, v2, v3, rk, so0 + (t >> 1) * 131072 + (t & 1) * 4096);
      if (t == 0) __builtin_amdgcn_s_barrier();
    }
    const unsigned ka = ring + n * 512 + ((g ^ ((n & 7) << 1)) << 4);
    u32x4 kf[4][8];
#pragma unroll
    for (int c = 0; c < 4; ++c) {
#pragma unroll
      for (int kk = 0; kk < 8; ++kk) kf[c][kk] = *(LAS u32x4*)(L + ((ka ^ ((kk & 3) * 64)) + (kk >> 2) * 256 + c * 8192));
      dma4(lds0 + ring + c * 8192, v0, v1, v2, v3, rk, so0 + (c + 4) * 131072);
      dma4(lds0 + ring + c * 8192 + 4096, v0, v1, v2, v3, rk, so0 + (c + 4) * 131072 + 4096);
    }
    STAMP(1);
    lds_wait_ge(L, F_Q, 1);
    STAMP(2);
    u32x4 qf[8];
#pragma unroll
    for (int kk = 0; kk < 8; ++kk) qf[kk] = *(LAS u32x4*)(L + L_Q + (n & 1) * 512 + kk * 64 + g * 16);
    LAS unsigned char* C = L + L_C + wave * 2048;
#pragma unroll
    for (int c = 0; c < 4; ++c) {
      f32x4 sc = {0.f, 0.f, 0.f, 0.f};
#pragma unroll
      for (int kk = 0; kk < 8; ++kk) sc = mfma(*(LAS u32x4*)(L + ((ka ^ ((kk & 3) * 64)) + (kk >> 2) * 256 + c * 8192)), qf[kk], sc);
      if (n < 2) *(LAS f32x4*)(C + (c + 4) * 128 + n * 64 + g * 16) = sc;
      dma4(lds0 + ring + c * 8192, v0, v1, v2, v3, rv, so0 + c * 131072);
      sc = f32x4{0.f, 0.f, 0.f, 0.f};
#pragma unroll
      for (int kk = 0; kk < 8; ++kk) sc = mfma(kf[c][kk], qf[kk], sc);
      if (n < 2) *(LAS f32x4*)(C + c * 128 + n * 64 + g * 16) = sc;
      dma4(lds0 + ring + c * 8192 + 4096, v0, v1, v2, v3, rv, so0 + c * 131072 + 4096);
    }
    STAMP(3);
    {
      const f32x4 sc = *(LAS f32x4*)(C + lane * 16);
      float m = fmaxf(fmaxf(sc[0], sc[1]), fmaxf(sc[2], sc[3]));
      m = fmaxf(m, dpp<D_X1>(m)); m = fmaxf(m, dpp<D_X2>(m)); m = fmaxf(m, dpp<D_R8>(m)); m = xrow_max(m);
      const float p0 = __builtin_amdgcn_exp2f((sc[0] - m) * SCL), p1 = __builtin_amdgcn_exp2f((sc[1] - m) * SCL),
                  p2 = __builtin_amdgcn_exp2f((sc[2] - m) * SCL), p3 = __builtin_amdgcn_exp2f((sc[3] - m) * SCL);
      float l = (p0 + p1) + (p2 + p3);
      l += dpp<D_X1>(l); l += dpp<D_X2>(l); l += dpp<D_R8>(l); l = xrow_sum(l);
      *(LAS u32x2*)(C + 1024 + lane * 8) = u32x2{pk(p0, p1), pk(p2, p3)};
      if ((lane & ~4) == 0) {
        *(LAS float*)(L + L_STL + wave * 16 + lane) = m;
        *(LAS float*)(L + L_STL + wave * 16 + 8 + lane) = l;
      }
      WAITLGKM0();
      if (lane == 0) __hip_atomic_fetch_add((LAS unsigned*)(L + F_ST), 1u, __ATOMIC_RELAXED, __HIP_MEMORY_SCOPE_WORKGROUP);
    }
    f32x4 acc[8];
#pragma unroll
    for (int d = 0; d < 8; ++d) acc[d] = f32x4{0.f, 0.f, 0.f, 0.f};
    const unsigned vr = 4 * g + (n >> 2);
    const unsigned va = ring + vr * 512 + ((vr & 7) << 5) + (lane & 3) * 8;
    f32x4 f4 = {0.f, 0.f, 0.f, 0.f};
#pragma nounroll
    for (int c = 0; c < 8; ++c) {
      if (c == 7) {
        lds_wait_ge(L, F_F, 1);
        f4 = *(LAS f32x4*)(L + L_FF + (lane >> 5) * 16);
        STAMP(4);
        WAITVM0();
        STAMP(5);
      }
      const u32x2 p = *(LAS u32x2*)(C + 1024 + (c * 8 + (n & 1) * 4 + g) * 8);
      const bool up = (n & 2) != 0;
      const u32x4 pf = {up ? 0u : p[0], up ? 0u : p[1], up ? p[0] : 0u, up ? p[1] : 0u};
      const unsigned a = va + (c & 3) * 8192;
#pragma unroll
      for (int d = 0; d < 8; ++d) { LAS unsigned char* pa = L + (a ^ (d * 32)); acc[d] = mfma(trfrag(pa, pa + 256), pf, acc[d]); }
      if (c < 4) issue(c + 12);
    }
    if (n < 4) {
#pragma unroll
      for (int d = 0; d < 8; ++d) *(LAS f32x4*)(C + (2 * (n & 1) + (n >> 1)) * 512 + d * 64 + g * 16) = acc[d];
    }
    WAITLGKM0();
    unsigned old = 0;
    if (lane == 0) old = __hip_atomic_fetch_add((LAS unsigned*)(L + F_ARR), 1u, __ATOMIC_RELAXED, __HIP_MEMORY_SCOPE_WORKGROUP);
    old = __builtin_amdgcn_readfirstlane(old);
    CFENCE();
    if (old == 3) {
      f32x4 o0 = {0.f, 0.f, 0.f, 0.f}, o1 = {0.f, 0.f, 0.f, 0.f};
#pragma unroll
      for (int w = 0; w < 4; ++w) {
        o0 += f4[w] * *(LAS f32x4*)(L + L_C + w * 2048 + lane * 32);
        o1 += f4[w] * *(LAS f32x4*)(L + L_C + w * 2048 + lane * 32 + 16);
      }
      const u32x4 ov = {nosent(pk(o0[0], o0[1])), nosent(pk(o0[2], o0[3])), nosent(pk(o1[0], o1[1])), nosent(pk(o1[2], o1[3]))};
      ST128(ov, WS_O + (hk * 32 + r) * 1024 + lane * 16);
    }
    STAMP(6);
    STAMP_OUT();
    return;
  }

  STAMP(0);
  const int j = s, cg = b;
  const unsigned tp = (lane & 3) * 8;
  const unsigned vrow = lane >> 3, vcol = (lane & 7) * 16;
  const unsigned stoff = WS_ST + ((hk * 8 + b) * 16) * 16;
  const unsigned pbase = WS_P + (cg * 32 + hk * 4 + j) * 2048;
  const unsigned hbase = WS_H + (hk * 4 + j) * 1024;
  if (lane < 4) *(LAS unsigned*)(L + L_F + lane * 4) = 0u;
  WAITLGKM0();
  __builtin_amdgcn_s_barrier();
  u32x2 ep = __builtin_amdgcn_raw_buffer_load_b64(rws, WS_E, 0, 16);
  {
    const u32x4 rq = make_srd(wq, 448 * 8192), rx = make_srd(x, 7168), rg = make_srd(gw, 896);
#pragma nounroll
    for (int i = 0; i < 14; ++i) dma1(lds0 + L_A + i * 1024, vrow * 8192 + vcol, rq, (112 * s + 8 * i) * 8192 + hk * 1024 + b * 128);
#pragma nounroll
    for (int i = 0; i < 7; ++i) dma1w(lds0 + L_X + i * 1024, lane * 16, rx, i * 1024);
    dma1w(lds0 + L_G, lane * 16, rg, 0);
  }
  {
    const u32x4 sv = {SENT, SENT, SENT, SENT};
    ST128(sv, WS_O + (hk * 32 + r) * 1024 + lane * 16);
    ST128(sv, r < 28 ? pbase + lane * 16 : hbase + lane * 16);
    ST128(sv, r < 28 ? pbase + 1024 + lane * 16 : OOB);
    ST128(sv, lane < 4 ? stoff + (s * 4 + lane) * 16 : OOB);
  }
  asm volatile("" : "+v"(ep));
  const unsigned t0 = __builtin_amdgcn_readfirstlane(ep[0]) + 1u;
  const unsigned t1 = __builtin_amdgcn_readfirstlane(ep[1]) + (t0 == 0u ? 1u : 0u);
  WAITVM0();
  STAMP(1);
  f32x4 acc[4];
  auto proj = [&](int k0) {
#pragma unroll
    for (int ks = 0; ks < 7; ++ks) {
      const u32x2 xf = *(LAS u32x2*)(L + L_X + (n & 7) * 896 + (k0 + 16 * ks + 4 * g) * 2);
#pragma unroll
      for (int mt = 0; mt < 4; ++mt) acc[mt] = mfma16(trread(L + L_A + (16 * ks + 4 * g + (n >> 2)) * 128 + 32 * mt + tp), xf, acc[mt]);
    }
  };
#pragma unroll
  for (int mt = 0; mt < 4; ++mt) acc[mt] = f32x4{0.f, 0.f, 0.f, 0.f};
  {
    proj(112 * s);
    const unsigned qo = WS_Q + ((((n & 7) * 8 + hk) * 4 + s) * 512 + 64 * b + 16 * (n >> 3) + 4 * g) * 4;
    ST128(__builtin_bit_cast(u32x4, n < 8 ? acc[0] : acc[1]), qo);
    ST128(__builtin_bit_cast(u32x4, n < 8 ? acc[2] : acc[3]), qo + 128);
  }
  float gate;
  {
    f32x4 d = {0.f, 0.f, 0.f, 0.f};
#pragma unroll
    for (int kk = 0; kk < 14; ++kk) d = mfma(*(LAS u32x4*)(L + L_G + kk * 64 + g * 16), *(LAS u32x4*)(L + L_X + (n & 7) * 896 + kk * 64 + g * 16), d);
    gate = __builtin_amdgcn_rcpf(1.f + __expf(-d[0]));
  }
  WAITVM0();
  ST64((u32x2{t0, t1}), lane == 0 ? WS_FLAG + (hk * 32 + r) * 8 : OOB);
  WAITLGKM0();
  STAMP(2);
  const u32x4 r1 = make_srd(w1, 448 * 3584);
  const bool ffn = hk < 7;
  if (ffn) {
    if (r < 28) {
      const u32x4 r2 = make_srd(w2, 1792 * 896);
#pragma nounroll
      for (int i = 0; i < 8; ++i) dma1w(lds0 + L_A + i * 1024, vrow * 896 + vcol, r2, (64 * (hk * 4 + j) + 8 * i) * 896 + cg * 128);
    } else {
#pragma nounroll
      for (int i = 0; i < 14; ++i) dma1w(lds0 + L_A + i * 1024, vrow * 3584 + vcol, r1, 8 * i * 3584 + (hk * 4 + j) * 128);
    }
  }
  {
#pragma nounroll
    for (int it = 0; it < SPIN_MEM; ++it) {
      const u32x4 a = LD128(WS_FLAG + hk * 256 + n * 16);
      if (__builtin_amdgcn_ballot_w64(((a[0] ^ t0) | (a[1] ^ t1) | (a[2] ^ t0) | (a[3] ^ t1)) != 0) == 0) break;
      __builtin_amdgcn_s_sleep(16);
      CFENCE();
    }
    CFENCE();
    STAMP(3);
    u32x4 v[8];
#pragma unroll
    for (int i = 0; i < 8; ++i) v[i] = LD128(WS_Q + (b * 8 + hk) * 8192 + i * 1024 + lane * 16);
#pragma unroll
    for (int h = 0; h < 2; ++h) {
      const f32x4 q = (__builtin_bit_cast(f32x4, v[h]) + __builtin_bit_cast(f32x4, v[2 + h])) + (__builtin_bit_cast(f32x4, v[4 + h]) + __builtin_bit_cast(f32x4, v[6 + h]));
      *(LAS u32x2*)(L + L_Q + h * 512 + lane * 8) = u32x2{pk(q[0], q[1]), pk(q[2], q[3])};
    }
    WAITLGKM0();
    *(volatile LAS unsigned*)(L + F_Q) = 1u;
  }
  WAITVM0();
  STAMP(4);
#pragma unroll
  for (int mt = 0; mt < 4; ++mt) acc[mt] = f32x4{0.f, 0.f, 0.f, 0.f};
  u32x4 aw[4][4], a2[2][4];
  if (r >= 28) {
    if (ffn) {
#pragma nounroll
      for (int kq = 0; kq < 4; ++kq) {
        proj(112 * kq);
        if (kq < 3) {
          WAITLGKM0();
#pragma nounroll
          for (int i = 0; i < 14; ++i) dma1w(lds0 + L_A + i * 1024, vrow * 3584 + vcol, r1, (112 * (kq + 1) + 8 * i) * 3584 + (hk * 4 + j) * 128);
          WAITVM0();
        }
      }
#pragma unroll
      for (int mt = 0; mt < 4; ++mt) {
        float hv[4];
#pragma unroll
        for (int i = 0; i < 4; ++i) { const float t = rbf(acc[mt][i]); hv[i] = (1.f - gate) * t * __builtin_amdgcn_rcpf(1.f + __expf(-t)); }
        ST64((u32x2{nosent(pk(hv[0], hv[1])), nosent(pk(hv[2], hv[3]))}), n < 8 ? hbase + n * 128 + mt * 32 + g * 8 : OOB);
      }
    }
  } else {
#pragma unroll
    for (int kk = 0; kk < 2; ++kk)
#pragma unroll
      for (int mt = 0; mt < 4; ++mt) { LAS unsigned char* pa = L + L_A + (32 * kk + 8 * g + (n >> 2)) * 128 + 32 * mt + tp; a2[kk][mt] = trfrag(pa, pa + 512); }
    WAITLGKM0();
    {
      const u32x4 ro = make_srd(wo, 4096 * 896);
#pragma nounroll
      for (int i = 0; i < 16; ++i) dma1w(lds0 + L_A + i * 1024, vrow * 896 + vcol, ro, (512 * hk + 128 * j + 8 * i) * 896 + cg * 128);
      WAITVM0();
    }
#pragma unroll
    for (int kk = 0; kk < 4; ++kk)
#pragma unroll
      for (int mt = 0; mt < 4; ++mt) { LAS unsigned char* pa = L + L_A + (32 * kk + 8 * g + (n >> 2)) * 128 + 32 * mt + tp; aw[kk][mt] = trfrag(pa, pa + 512); }
    STAMP(5);
  }
  STAMP(6);
  {
    lds_wait_ge(L, F_ST, 4);
    const u32x4 own = *(LAS u32x4*)(L + L_STL + (lane & 3) * 16);
    ST128((u32x4{nosent(own[0]), nosent(own[1]), nosent(own[2]), nosent(own[3])}), lane < 4 ? stoff + (s * 4 + lane) * 16 : OOB);
    u32x4 v;
#pragma nounroll
    for (int it = 0; it < SPIN_MEM; ++it) {
      v = LD128(lane < 16 ? stoff + lane * 16 : OOB);
      if (__builtin_amdgcn_ballot_w64(umax4(v) == SENT) == 0) break;
      __builtin_amdgcn_s_sleep(16);
      CFENCE();
    }
    const float gb = asf(__builtin_amdgcn_readlane(asu(gate), b));
#pragma unroll
    for (int h = 0; h < 2; ++h) {
      const float m = asf(v[h]);
      float M = fmaxf(m, dpp<D_X1>(m)); M = fmaxf(M, dpp<D_X2>(M)); M = fmaxf(M, dpp<D_R4>(M)); M = fmaxf(M, dpp<D_R8>(M));
      const float e = __builtin_amdgcn_exp2f((m - M) * SCL);
      float l = asf(v[2 + h]) * e; l += dpp<D_X1>(l); l += dpp<D_X2>(l); l += dpp<D_R4>(l); l += dpp<D_R8>(l);
      if ((lane >> 2) == s) *(LAS float*)(L + L_FF + h * 16 + (lane & 3) * 4) = gb * e * __builtin_amdgcn_rcpf(l);
    }
    WAITLGKM0();
    *(volatile LAS unsigned*)(L + F_F) = 1u;
  }
  {
    const bool all = wg == 255;
    if (r >= 28 && !all) { STAMP_OUT(); return; }
    const unsigned fo = all ? WS_FLAG + lane * 32 : WS_FLAG + (lane & 7) * 256 + cg * 32;
#pragma nounroll
    for (int it = 0; it < SPIN_MEM; ++it) {
      const u32x4 a = LD128(fo), c = LD128(fo + 16);
      const unsigned bad = (a[0] ^ t0) | (a[1] ^ t1) | (a[2] ^ t0) | (a[3] ^ t1) | (c[0] ^ t0) | (c[1] ^ t1) | (c[2] ^ t0) | (c[3] ^ t1);
      if (__builtin_amdgcn_ballot_w64(bad != 0) == 0) break;
      __builtin_amdgcn_s_sleep(32);
      CFENCE();
    }
    CFENCE();
    if (all) { ST64((u32x2{t0, t1}), lane == 0 ? WS_E : OOB); STAMP_OUT(); return; }
  }
    if (ffn) {
      const unsigned ho = n < 8 ? hbase + n * 128 + g * 16 : OOB;
      u32x4 h0, h1;
#pragma nounroll
      for (int it = 0; it < SPIN_MEM; ++it) {
        h0 = LD128(ho); h1 = LD128(ho + 64);
        if (__builtin_amdgcn_ballot_w64(umax(umax4(h0), umax4(h1)) == SENT) == 0) break;
        __builtin_amdgcn_s_sleep(32);
        CFENCE();
      }
#pragma unroll
      for (int mt = 0; mt < 4; ++mt) acc[mt] = mfma(a2[1][mt], h1, mfma(a2[0][mt], h0, acc[mt]));
    }
  STAMP(7);
  lds_wait_ge(L, F_ARR, 4);
  STAMP(8);
  {
    const unsigned oo = WS_O + (hk * 32 + 4 * (n & 7) + (n >> 3)) * 1024 + j * 256 + g * 16;
    u32x4 bo[4][2];
#pragma nounroll
    for (int it = 0; it < SPIN_MEM; ++it) {
      unsigned mx = 0;
#pragma unroll
      for (int kk = 0; kk < 4; ++kk)
#pragma unroll
        for (int t = 0; t < 2; ++t) bo[kk][t] = LD128(oo + t * 2048 + kk * 64);
#pragma unroll
      for (int kk = 0; kk < 4; ++kk)
#pragma unroll
        for (int t = 0; t < 2; ++t) mx = umax(mx, umax4(bo[kk][t]));
      if (__builtin_amdgcn_ballot_w64(mx == SENT) == 0) break;
      CFENCE();
    }
    STAMP(9);
#pragma unroll
    for (int mt = 0; mt < 4; ++mt) {
#pragma unroll
      for (int kk = 0; kk < 4; ++kk)
#pragma unroll
        for (int t = 0; t < 2; ++t) acc[mt] = mfma(aw[kk][mt], bo[kk][t], acc[mt]);
      u32x4 o;
#pragma unroll
      for (int i = 0; i < 4; ++i) o[i] = nosent(asu(acc[mt][i] + dpp<D_R8>(acc[mt][i])));
      ST128(o, n < 8 ? pbase + ((4 * mt + g) * 8 + n) * 16 : OOB);
    }
  }
  STAMP(10);
  {
    const int rr = hk * 4 + j;
    const unsigned po = WS_P + (cg * 32 + (lane >> 2)) * 2048 + (rr * 4 + (lane & 3)) * 16;
    u32x4 a, c;
#pragma nounroll
    for (int it = 0; it < SPIN_MEM; ++it) {
      a = LD128(po); c = LD128(po + 16 * 2048);
      if (__builtin_amdgcn_ballot_w64(umax(umax4(a), umax4(c)) == SENT) == 0) break;
      CFENCE();
    }
    STAMP(11);
    float o[4];
#pragma unroll
    for (int i = 0; i < 4; ++i) { float t = asf(a[i]) + asf(c[i]); t += dpp<D_R4>(t); t += dpp<D_R8>(t); o[i] = xrow_sum(t); }
    if (lane < 4) *(u32x2*)(out + (4 * (rr & 1) + lane) * 448 + 64 * cg + 4 * (rr >> 1)) = u32x2{pk(o[0], o[1]), pk(o[2], o[3])};
  }
  STAMP(12);
  STAMP_OUT();
}

extern "C" void kernel_launch(void* const* d_in, const int* in_sizes, int n_in, void* d_out, int out_size, void* d_ws, size_t ws_size,
                              hipStream_t stream) {
  gated_mix_kernel<<<256, 320, 0, stream>>>((const unsigned short*)d_in[0], (const unsigned short*)d_in[1], (const unsigned short*)d_in[2],
                                            (const unsigned short*)d_in[3], (const unsigned short*)d_in[4], (const unsigned short*)d_in[6],
                                            (const unsigned short*)d_in[7], (const unsigned short*)d_in[8], (unsigned short*)d_out,
                                            (unsigned char*)d_ws);
}
```

```cpp
#define DIAG_I 15
#define DIAG_ROLE 0
#define DIAG_WG -1
#include <hip/hip_runtime.h>
#include <hip/hip_bf16.h>
#include <stdint.h>

#define LAS __attribute__((address_space(3)))
typedef unsigned u32x4 __attribute__((ext_vector_type(4)));
typedef unsigned u32x2 __attribute__((ext_vector_type(2)));
typedef float f32x4 __attribute__((ext_vector_type(4)));
typedef float f32x2 __attribute__((ext_vector_type(2)));
typedef __bf16 bf16x8 __attribute__((ext_vector_type(8)));
typedef __bf16 bf16x2 __attribute__((ext_vector_type(2)));
typedef short s16x4 __attribute__((ext_vector_type(4)));
typedef unsigned long long u64;

#define INFL 7

constexpr int SK = 8192, WIN = 2048;
constexpr unsigned WS_E = 0, WS_FLAG = 4096, WS_ST = 8192, WS_H = 32768, WS_Q = 65536, WS_O = WS_Q + 524288, WS_P = WS_O + 262144,
                   WS_END = WS_P + 224 * 2048;
constexpr unsigned OOB = 0x7ffffff0u;
constexpr unsigned SENT = 0xffffffffu;
constexpr unsigned L_A = 131072, L_X = L_A + 14336, L_G = L_X + 7168, L_C = L_G + 1024, L_Q = L_C + 8192, L_F = L_Q + 1024, L_END = L_F + 256;
constexpr unsigned F_Q = L_F, F_ST = L_F + 4, F_F = L_F + 8, F_ARR = L_F + 12, L_STL = L_F + 64, L_FF = L_F + 128;
constexpr int SPIN_LDS = 1 << 20, SPIN_MEM = 1 << 16;
constexpr float SCL = 0.0625f * 1.4426950408889634f;

__device__ __forceinline__ u32x4 make_srd(const void* p, unsigned bytes) {
  const u64 a = (u64)p;
  u32x4 r; r[0] = (unsigned)a; r[1] = (unsigned)(a >> 32) & 0xffffu; r[2] = bytes; r[3] = 0x00020000u; return r;
}
__device__ __forceinline__ void dma1(unsigned ldsaddr, unsigned voff, u32x4 srd, unsigned soff) {
  asm volatile("s_mov_b32 m0, %0\n\ts_nop 0\n\tbuffer_load_dwordx4 %1, %2, %3 offen nt lds" ::"s"(ldsaddr), "v"(voff), "s"(srd), "s"(soff) : "memory");
}
#define WPOL "nt "
#define WINFL 15
__device__ __forceinline__ void dma1w(unsigned ldsaddr, unsigned voff, u32x4 srd, unsigned soff) {
  asm volatile("s_mov_b32 m0, %0\n\ts_waitcnt vmcnt(%4)\n\tbuffer_load_dwordx4 %1, %2, %3 offen " WPOL "lds" ::"s"(ldsaddr), "v"(voff), "s"(srd), "s"(soff), "i"(WINFL) : "memory");
}
__device__ __forceinline__ void dma4(unsigned ldsaddr, unsigned v0, unsigned v1, unsigned v2, unsigned v3, u32x4 srd, unsigned soff) {
  asm volatile("s_mov_b32 m0, %0\n\t"
               "s_waitcnt vmcnt(%7) lgkmcnt(0)\n\tbuffer_load_dwordx4 %1, %5, %6 offen nt lds\n\t"
               "s_waitcnt vmcnt(%7)\n\tbuffer_load_dwordx4 %2, %5, %6 offen offset:1024 nt lds\n\t"
               "s_waitcnt vmcnt(%7)\n\tbuffer_load_dwordx4 %3, %5, %6 offen offset:2048 nt lds\n\t"
               "s_waitcnt vmcnt(%7)\n\tbuffer_load_dwordx4 %4, %5, %6 offen offset:3072 nt lds"
               ::"s"(ldsaddr), "v"(v0), "v"(v1), "v"(v2), "v"(v3), "s"(srd), "s"(soff), "i"(INFL - 1) : "memory");
}
#define WAITVM0() asm volatile("s_waitcnt vmcnt(0)" ::: "memory")
#define WAITLGKM0() asm volatile("s_waitcnt lgkmcnt(0)" ::: "memory")
#define CFENCE() asm volatile("" ::: "memory")

__device__ __forceinline__ f32x4 mfma(u32x4 a, u32x4 b, f32x4 c) {
  return __builtin_amdgcn_mfma_f32_16x16x32_bf16(__builtin_bit_cast(bf16x8, a), __builtin_bit_cast(bf16x8, b), c, 0, 0, 0);
}
__device__ __forceinline__ f32x4 mfma16(u32x2 a, u32x2 b, f32x4 c) {
  return __builtin_amdgcn_mfma_f32_16x16x16bf16_1k(__builtin_bit_cast(s16x4, a), __builtin_bit_cast(s16x4, b), c, 0, 0, 0);
}
__device__ __forceinline__ u32x2 trread(LAS unsigned char* p) {
  return __builtin_bit_cast(u32x2, __builtin_amdgcn_ds_read_tr16_b64_v4i16((LAS s16x4*)p));
}
__device__ __forceinline__ u32x4 trfrag(LAS unsigned char* p0, LAS unsigned char* p1) {
  const u32x2 a = trread(p0), b = trread(p1); return u32x4{a[0], a[1], b[0], b[1]};
}
__device__ __forceinline__ unsigned pk(float lo, float hi) {
  const bf16x2 v = {(__bf16)lo, (__bf16)hi}; return __builtin_bit_cast(unsigned, v);
}
__device__ __forceinline__ float rbf(float f) { return (float)(__bf16)f; }
__device__ __forceinline__ float asf(unsigned u) { return __builtin_bit_cast(float, u); }
__device__ __forceinline__ unsigned asu(float f) { return __builtin_bit_cast(unsigned, f); }
__device__ __forceinline__ unsigned nosent(unsigned u) { return u < SENT - 1u ? u : SENT - 1u; }
__device__ __forceinline__ unsigned umax(unsigned a, unsigned b) { return a > b ? a : b; }
__device__ __forceinline__ unsigned umax4(u32x4 v) { return umax(umax(v[0], v[1]), umax(v[2], v[3])); }
template <int CTRL> __device__ __forceinline__ float dpp(float x) {
  return __builtin_bit_cast(float, __builtin_amdgcn_mov_dpp(__builtin_bit_cast(int, x), CTRL, 0xf, 0xf, true));
}
constexpr int D_X1 = 0xB1, D_X2 = 0x4E, D_R4 = 0x124, D_R8 = 0x128;
__device__ __forceinline__ float xrow_max(float x) {
  auto s = __builtin_amdgcn_permlane16_swap(asu(x), asu(x), false, false); x = fmaxf(asf(s[0]), asf(s[1]));
  auto t = __builtin_amdgcn_permlane32_swap(asu(x), asu(x), false, false); return fmaxf(asf(t[0]), asf(t[1]));
}
__device__ __forceinline__ float xrow_sum(float x) {
  auto s = __builtin_amdgcn_permlane16_swap(asu(x), asu(x), false, false); x = asf(s[0]) + asf(s[1]);
  auto t = __builtin_amdgcn_permlane32_swap(asu(x), asu(x), false, false); return asf(t[0]) + asf(t[1]);
}
__device__ __forceinline__ void lds_wait_ge(LAS unsigned char* L, unsigned off, unsigned want) {
#pragma nounroll
  for (int i = 0; i < SPIN_LDS; ++i) {
    if ((unsigned)__builtin_amdgcn_readfirstlane(*(volatile LAS unsigned*)(L + off)) >= want) break;
    __builtin_amdgcn_s_sleep(1);
  }
  CFENCE();
}
#define LD128(off) __builtin_amdgcn_raw_buffer_load_b128(rws, (off), 0, 16)
#define ST128(v, off) __builtin_amdgcn_raw_buffer_store_b128((v), rws, (off), 0, 16)
#define ST64(v, off) __builtin_amdgcn_raw_buffer_store_b64((v), rws, (off), 0, 16)

__device__ __forceinline__ unsigned nowlo() { u64 t_; asm volatile("s_memrealtime %0\n\ts_waitcnt lgkmcnt(0)" : "=s"(t_) :: "memory"); return (unsigned)t_; }
#define STAMP(i) do { if ((i) == DIAG_I && (wave == 4) == DIAG_ROLE && (DIAG_WG < 0 || wg == DIAG_WG)) dgx = nowlo() - dg0; } while (0)
#define STAMP_OUT() do { for (int sp_ = 0; sp_ < 1000000 && nowlo() - dg0 < 5000 + 4 * dgx; ++sp_) __builtin_amdgcn_s_sleep(2); } while (0)
#define STAMP_DECL() unsigned dgx = 0

__global__ void __launch_bounds__(320, 2)
gated_mix_kernel(const unsigned short* x, const unsigned short* kc, const unsigned short* vc,
                 const unsigned short* wq, const unsigned short* wo, const unsigned short* w1,
                 const unsigned short* w2, const unsigned short* gw, unsigned short* out,
                 unsigned char* ws) {
  __shared__ __attribute__((aligned(16))) unsigned char smem[L_END];
  LAS unsigned char* L = (LAS unsigned char*)smem;
  const unsigned lds0 = (unsigned)(size_t)L;
  const unsigned dg0 = nowlo();
  asm volatile("" ::"s"(x), "s"(kc), "s"(vc), "s"(wq), "s"(gw), "s"(ws));
  const int tid = threadIdx.x, lane = tid & 63, wave = __builtin_amdgcn_readfirstlane(tid >> 6);
  const int wg = blockIdx.x, hk = wg & 7, r = wg >> 3, b = r >> 2, s = r & 3;
  const int g = lane >> 4, n = lane & 15;
  const __amdgpu_buffer_rsrc_t rws = __builtin_amdgcn_make_buffer_rsrc((void*)ws, 0, (int)WS_END, 0x00020000);

  STAMP_DECL();
  if (wave < 4) {
    const size_t wb = ((size_t)(b * 8 + hk) * SK + (SK - WIN)) * 256;
    const u32x4 rk = make_srd(kc + wb, WIN * 512), rv = make_srd(vc + wb, WIN * 512);
    const unsigned hi = lane >> 5, vb = hi * 512 + (((lane & 31) ^ (hi << 1)) << 4);
    const unsigned v0 = vb, v1 = vb ^ 64, v2 = vb ^ 128, v3 = vb ^ 192;
    const unsigned ring = wave * 32768, so0 = (s * 4 + wave) * 8192;
    auto issue = [&](int t) {
      const u32x4 rs = t < 8 ? rk : rv;
      const unsigned so = so0 + (t & 7) * 131072, m = lds0 + ring + (t & 3) * 8192;
      dma4(m, v0, v1, v2, v3, rs, so);
      dma4(m + 4096, v0, v1, v2, v3, rs, so + 4096);
    };
    STAMP(0);
#pragma nounroll
    for (int t = 0; t < 8; ++t) {
      dma4(lds0 + ring + t * 4096, v0, v1, v2, v3, rk, so0 + (t >> 1) * 131072 + (t & 1) * 4096);
      if (t == 0) __builtin_amdgcn_s_barrier();
    }
    const unsigned ka = ring + n * 512 + ((g ^ ((n & 7) << 1)) << 4);
    u32x4 kf[4][8];
#pragma unroll
    for (int c = 0; c < 4; ++c) {
#pragma unroll
      for (int kk = 0; kk < 8; ++kk) kf[c][kk] = *(LAS u32x4*)(L + ((ka ^ ((kk & 3) * 64)) + (kk >> 2) * 256 + c * 8192));
      dma4(lds0 + ring + c * 8192, v0, v1, v2, v3, rk, so0 + (c + 4) * 131072);
      dma4(lds0 + ring + c * 8192 + 4096, v0, v1, v2, v3, rk, so0 + (c + 4) * 131072 + 4096);
    }
    STAMP(1);
    lds_wait_ge(L, F_Q, 1);
    STAMP(2);
    u32x4 qf[8];
#pragma unroll
    for (int kk = 0; kk < 8; ++kk) qf[kk] = *(LAS u32x4*)(L + L_Q + (n & 1) * 512 + kk * 64 + g * 16);
    LAS unsigned char* C = L + L_C + wave * 2048;
#pragma unroll
    for (int c = 0; c < 4; ++c) {
      f32x4 sc = {0.f, 0.f, 0.f, 0.f};
#pragma unroll
      for (int kk = 0; kk < 8; ++kk) sc = mfma(*(LAS u32x4*)(L + ((ka ^ ((kk & 3) * 64)) + (kk >> 2) * 256 + c * 8192)), qf[kk], sc);
      if (n < 2) *(LAS f32x4*)(C + (c + 4) * 128 + n * 64 + g * 16) = sc;
      dma4(lds0 + ring + c * 8192, v0, v1, v2, v3, rv, so0 + c * 131072);
      sc = f32x4{0.f, 0.f, 0.f, 0.f};
#pragma unroll
      for (int kk = 0; kk < 8; ++kk) sc = mfma(kf[c][kk], qf[kk], sc);
      if (n < 2) *(LAS f32x4*)(C + c * 128 + n * 64 + g * 16) = sc;
      dma4(lds0 + ring + c * 8192 + 4096, v0, v1, v2, v3, rv, so0 + c * 131072 + 4096);
    }
    STAMP(3);
    {
      const f32x4 sc = *(LAS f32x4*)(C + lane * 16);
      float m = fmaxf(fmaxf(sc[0], sc[1]), fmaxf(sc[2], sc[3]));
      m = fmaxf(m, dpp<D_X1>(m)); m = fmaxf(m, dpp<D_X2>(m)); m = fmaxf(m, dpp<D_R8>(m)); m = xrow_max(m);
      const float p0 = __builtin_amdgcn_exp2f((sc[0] - m) * SCL), p1 = __builtin_amdgcn_exp2f((sc[1] - m) * SCL),
                  p2 = __builtin_amdgcn_exp2f((sc[2] - m) * SCL), p3 = __builtin_amdgcn_exp2f((sc[3] - m) * SCL);
      float l = (p0 + p1) + (p2 + p3);
      l += dpp<D_X1>(l); l += dpp<D_X2>(l); l += dpp<D_R8>(l); l = xrow_sum(l);
      *(LAS u32x2*)(C + 1024 + lane * 8) = u32x2{pk(p0, p1), pk(p2, p3)};
      if ((lane & ~4) == 0) {
        *(LAS float*)(L + L_STL + wave * 16 + lane) = m;
        *(LAS float*)(L + L_STL + wave * 16 + 8 + lane) = l;
      }
      WAITLGKM0();
      if (lane == 0) __hip_atomic_fetch_add((LAS unsigned*)(L + F_ST), 1u, __ATOMIC_RELAXED, __HIP_MEMORY_SCOPE_WORKGROUP);
    }
    f32x4 acc[8];
#pragma unroll
    for (int d = 0; d < 8; ++d) acc[d] = f32x4{0.f, 0.f, 0.f, 0.f};
    const unsigned vr = 4 * g + (n >> 2);
    const unsigned va = ring + vr * 512 + ((vr & 7) << 5) + (lane & 3) * 8;
    f32x4 f4 = {0.f, 0.f, 0.f, 0.f};
#pragma nounroll
    for (int c = 0; c < 8; ++c) {
      if (c == 7) {
        lds_wait_ge(L, F_F, 1);
        f4 = *(LAS f32x4*)(L + L_FF + (lane >> 5) * 16);
        STAMP(4);
        WAITVM0();
        STAMP(5);
      }
      const u32x2 p = *(LAS u32x2*)(C + 1024 + (c * 8 + (n & 1) * 4 + g) * 8);
      const bool up = (n & 2) != 0;
      const u32x4 pf = {up ? 0u : p[0], up ? 0u : p[1], up ? p[0] : 0u, up ? p[1] : 0u};
      const unsigned a = va + (c & 3) * 8192;
#pragma unroll
      for (int d = 0; d < 8; ++d) { LAS unsigned char* pa = L + (a ^ (d * 32)); acc[d] = mfma(trfrag(pa, pa + 256), pf, acc[d]); }
      if (c < 4) issue(c + 12);
    }
    if (n < 4) {
#pragma unroll
      for (int d = 0; d < 8; ++d) *(LAS f32x4*)(C + (2 * (n & 1) + (n >> 1)) * 512 + d * 64 + g * 16) = acc[d];
    }
    WAITLGKM0();
    unsigned old = 0;
    if (lane == 0) old = __hip_atomic_fetch_add((LAS unsigned*)(L + F_ARR), 1u, __ATOMIC_RELAXED, __HIP_MEMORY_SCOPE_WORKGROUP);
    old = __builtin_amdgcn_readfirstlane(old);
    CFENCE();
    if (old == 3) {
      f32x4 o0 = {0.f, 0.f, 0.f, 0.f}, o1 = {0.f, 0.f, 0.f, 0.f};
#pragma unroll
      for (int w = 0; w < 4; ++w) {
        o0 += f4[w] * *(LAS f32x4*)(L + L_C + w * 2048 + lane * 32);
        o1 += f4[w] * *(LAS f32x4*)(L + L_C + w * 2048 + lane * 32 + 16);
      }
      const u32x4 ov = {nosent(pk(o0[0], o0[1])), nosent(pk(o0[2], o0[3])), nosent(pk(o1[0], o1[1])), nosent(pk(o1[2], o1[3]))};
      ST128(ov, WS_O + (hk * 32 + r) * 1024 + lane * 16);
    }
    STAMP(6);
    STAMP_OUT();
    return;
  }

  STAMP(0);
  const int j = s, cg = b;
  const unsigned tp = (lane & 3) * 8;
  const unsigned vrow = lane >> 3, vcol = (lane & 7) * 16;
  const unsigned stoff = WS_ST + ((hk * 8 + b) * 16) * 16;
  const unsigned pbase = WS_P + (cg * 32 + hk * 4 + j) * 2048;
  const unsigned hbase = WS_H + (hk * 4 + j) * 1024;
  if (lane < 4) *(LAS unsigned*)(L + L_F + lane * 4) = 0u;
  WAITLGKM0();
  __builtin_amdgcn_s_barrier();
  u32x2 ep = __builtin_amdgcn_raw_buffer_load_b64(rws, WS_E, 0, 16);
  {
    const u32x4 rq = make_srd(wq, 448 * 8192), rx = make_srd(x, 7168), rg = make_srd(gw, 896);
#pragma nounroll
    for (int i = 0; i < 14; ++i) dma1(lds0 + L_A + i * 1024, vrow * 8192 + vcol, rq, (112 * s + 8 * i) * 8192 + hk * 1024 + b * 128);
#pragma nounroll
    for (int i = 0; i < 7; ++i) dma1w(lds0 + L_X + i * 1024, lane * 16, rx, i * 1024);
    dma1w(lds0 + L_G, lane * 16, rg, 0);
  }
  {
    const u32x4 sv = {SENT, SENT, SENT, SENT};
    ST128(sv, WS_O + (hk * 32 + r) * 1024 + lane * 16);
    ST128(sv, r < 28 ? pbase + lane * 16 : hbase + lane * 16);
    ST128(sv, r < 28 ? pbase + 1024 + lane * 16 : OOB);
    ST128(sv, lane < 4 ? stoff + (s * 4 + lane) * 16 : OOB);
  }
  asm volatile("" : "+v"(ep));
  const unsigned t0 = __builtin_amdgcn_readfirstlane(ep[0]) + 1u;
  const unsigned t1 = __builtin_amdgcn_readfirstlane(ep[1]) + (t0 == 0u ? 1u : 0u);
  WAITVM0();
  STAMP(1);
  f32x4 acc[4];
  auto proj = [&](int k0) {
#pragma unroll
    for (int ks = 0; ks < 7; ++ks) {
      const u32x2 xf = *(LAS u32x2*)(L + L_X + (n & 7) * 896 + (k0 + 16 * ks + 4 * g) * 2);
#pragma unroll
      for (int mt = 0; mt < 4; ++mt) acc[mt] = mfma16(trread(L + L_A + (16 * ks + 4 * g + (n >> 2)) * 128 + 32 * mt + tp), xf, acc[mt]);
    }
  };
#pragma unroll
  for (int mt = 0; mt < 4; ++mt) acc[mt] = f32x4{0.f, 0.f, 0.f, 0.f};
  {
    proj(112 * s);
    const unsigned qo = WS_Q + ((((n & 7) * 8 + hk) * 4 + s) * 512 + 64 * b + 16 * (n >> 3) + 4 * g) * 4;
    ST128(__builtin_bit_cast(u32x4, n < 8 ? acc[0] : acc[1]), qo);
    ST128(__builtin_bit_cast(u32x4, n < 8 ? acc[2] : acc[3]), qo + 128);
  }
  float gate;
  {
    f32x4 d = {0.f, 0.f, 0.f, 0.f};
#pragma unroll
    for (int kk = 0; kk < 14; ++kk) d = mfma(*(LAS u32x4*)(L + L_G + kk * 64 + g * 16), *(LAS u32x4*)(L + L_X + (n & 7) * 896 + kk * 64 + g * 16), d);
    gate = __builtin_amdgcn_rcpf(1.f + __expf(-d[0]));
  }
  WAITVM0();
  ST64((u32x2{t0, t1}), lane == 0 ? WS_FLAG + (hk * 32 + r) * 8 : OOB);
  WAITLGKM0();
  STAMP(2);
  const u32x4 r1 = make_srd(w1, 448 * 3584);
  const bool ffn = hk < 7;
  if (ffn) {
    if (r < 28) {
      const u32x4 r2 = make_srd(w2, 1792 * 896);
#pragma nounroll
      for (int i = 0; i < 8; ++i) dma1w(lds0 + L_A + i * 1024, vrow * 896 + vcol, r2, (64 * (hk * 4 + j) + 8 * i) * 896 + cg * 128);
    } else {
#pragma nounroll
      for (int i = 0; i < 14; ++i) dma1w(lds0 + L_A + i * 1024, vrow * 3584 + vcol, r1, 8 * i * 3584 + (hk * 4 + j) * 128);
    }
  }
  {
#pragma nounroll
    for (int it = 0; it < SPIN_MEM; ++it) {
      const u32x4 a = LD128(WS_FLAG + hk * 256 + n * 16);
      if (__builtin_amdgcn_ballot_w64(((a[0] ^ t0) | (a[1] ^ t1) | (a[2] ^ t0) | (a[3] ^ t1)) != 0) == 0) break;
      __builtin_amdgcn_s_sleep(16);
      CFENCE();
    }
    CFENCE();
    STAMP(3);
    u32x4 v[8];
#pragma unroll
    for (int i = 0; i < 8; ++i) v[i] = LD128(WS_Q + (b * 8 + hk) * 8192 + i * 1024 + lane * 16);
#pragma unroll
    for (int h = 0; h < 2; ++h) {
      const f32x4 q = (__builtin_bit_cast(f32x4, v[h]) + __builtin_bit_cast(f32x4, v[2 + h])) + (__builtin_bit_cast(f32x4, v[4 + h]) + __builtin_bit_cast(f32x4, v[6 + h]));
      *(LAS u32x2*)(L + L_Q + h * 512 + lane * 8) = u32x2{pk(q[0], q[1]), pk(q[2], q[3])};
    }
    WAITLGKM0();
    *(volatile LAS unsigned*)(L + F_Q) = 1u;
  }
  WAITVM0();
  STAMP(4);
#pragma unroll
  for (int mt = 0; mt < 4; ++mt) acc[mt] = f32x4{0.f, 0.f, 0.f, 0.f};
  u32x4 aw[4][4], a2[2][4];
  if (r >= 28) {
    if (ffn) {
#pragma nounroll
      for (int kq = 0; kq < 4; ++kq) {
        proj(112 * kq);
        if (kq < 3) {
          WAITLGKM0();
#pragma nounroll
          for (int i = 0; i < 14; ++i) dma1w(lds0 + L_A + i * 1024, vrow * 3584 + vcol, r1, (112 * (kq + 1) + 8 * i) * 3584 + (hk * 4 + j) * 128);
          WAITVM0();
        }
      }
#pragma unroll
      for (int mt = 0; mt < 4; ++mt) {
        float hv[4];
#pragma unroll
        for (int i = 0; i < 4; ++i) { const float t = rbf(acc[mt][i]); hv[i] = (1.f - gate) * t * __builtin_amdgcn_rcpf(1.f + __expf(-t)); }
        ST64((u32x2{nosent(pk(hv[0], hv[1])), nosent(pk(hv[2], hv[3]))}), n < 8 ? hbase + n * 128 + mt * 32 + g * 8 : OOB);
      }
    }
  } else {
#pragma unroll
    for (int kk = 0; kk < 2; ++kk)
#pragma unroll
      for (int mt = 0; mt < 4; ++mt) { LAS unsigned char* pa = L + L_A + (32 * kk + 8 * g + (n >> 2)) * 128 + 32 * mt + tp; a2[kk][mt] = trfrag(pa, pa + 512); }
    WAITLGKM0();
    {
      const u32x4 ro = make_srd(wo, 4096 * 896);
#pragma nounroll
      for (int i = 0; i < 16; ++i) dma1w(lds0 + L_A + i * 1024, vrow * 896 + vcol, ro, (512 * hk + 128 * j + 8 * i) * 896 + cg * 128);
      WAITVM0();
    }
#pragma unroll
    for (int kk = 0; kk < 4; ++kk)
#pragma unroll
      for (int mt = 0; mt < 4; ++mt) { LAS unsigned char* pa = L + L_A + (32 * kk + 8 * g + (n >> 2)) * 128 + 32 * mt + tp; aw[kk][mt] = trfrag(pa, pa + 512); }
    STAMP(5);
  }
  STAMP(6);
  {
    lds_wait_ge(L, F_ST, 4);
    const u32x4 own = *(LAS u32x4*)(L + L_STL + (lane & 3) * 16);
    ST128((u32x4{nosent(own[0]), nosent(own[1]), nosent(own[2]), nosent(own[3])}), lane < 4 ? stoff + (s * 4 + lane) * 16 : OOB);
    u32x4 v;
#pragma nounroll
    for (int it = 0; it < SPIN_MEM; ++it) {
      v = LD128(lane < 16 ? stoff + lane * 16 : OOB);
      if (__builtin_amdgcn_ballot_w64(umax4(v) == SENT) == 0) break;
      __builtin_amdgcn_s_sleep(16);
      CFENCE();
    }
    const float gb = asf(__builtin_amdgcn_readlane(asu(gate), b));
#pragma unroll
    for (int h = 0; h < 2; ++h) {
      const float m = asf(v[h]);
      float M = fmaxf(m, dpp<D_X1>(m)); M = fmaxf(M, dpp<D_X2>(M)); M = fmaxf(M, dpp<D_R4>(M)); M = fmaxf(M, dpp<D_R8>(M));
      const float e = __builtin_amdgcn_exp2f((m - M) * SCL);
      float l = asf(v[2 + h]) * e; l += dpp<D_X1>(l); l += dpp<D_X2>(l); l += dpp<D_R4>(l); l += dpp<D_R8>(l);
      if ((lane >> 2) == s) *(LAS float*)(L + L_FF + h * 16 + (lane & 3) * 4) = gb * e * __builtin_amdgcn_rcpf(l);
    }
    WAITLGKM0();
    *(volatile LAS unsigned*)(L + F_F) = 1u;
  }
  {
    const bool all = wg == 255;
    if (r >= 28 && !all) { STAMP_OUT(); return; }
    const unsigned fo = all ? WS_FLAG + lane * 32 : WS_FLAG + (lane & 7) * 256 + cg * 32;
#pragma nounroll
    for (int it = 0; it < SPIN_MEM; ++it) {
      const u32x4 a = LD128(fo), c = LD128(fo + 16);
      const unsigned bad = (a[0] ^ t0) | (a[1] ^ t1) | (a[2] ^ t0) | (a[3] ^ t1) | (c[0] ^ t0) | (c[1] ^ t1) | (c[2] ^ t0) | (c[3] ^ t1);
      if (__builtin_amdgcn_ballot_w64(bad != 0) == 0) break;
      __builtin_amdgcn_s_sleep(32);
      CFENCE();
    }
    CFENCE();
    if (all) { ST64((u32x2{t0, t1}), lane == 0 ? WS_E : OOB); STAMP_OUT(); return; }
  }
    if (ffn) {
      const unsigned ho = n < 8 ? hbase + n * 128 + g * 16 : OOB;
      u32x4 h0, h1;
#pragma nounroll
      for (int it = 0; it < SPIN_MEM; ++it) {
        h0 = LD128(ho); h1 = LD128(ho + 64);
        if (__builtin_amdgcn_ballot_w64(umax(umax4(h0), umax4(h1)) == SENT) == 0) break;
        __builtin_amdgcn_s_sleep(32);
        CFENCE();
      }
#pragma unroll
      for (int mt = 0; mt < 4; ++mt) acc[mt] = mfma(a2[1][mt], h1, mfma(a2[0][mt], h0, acc[mt]));
    }
  STAMP(7);
  lds_wait_ge(L, F_ARR, 4);
  STAMP(8);
  {
    const unsigned oo = WS_O + (hk * 32 + 4 * (n & 7) + (n >> 3)) * 1024 + j * 256 + g * 16;
    u32x4 bo[4][2];
#pragma nounroll
    for (int it = 0; it < SPIN_MEM; ++it) {
      unsigned mx = 0;
#pragma unroll
      for (int kk = 0; kk < 4; ++kk)
#pragma unroll
        for (int t = 0; t < 2; ++t) bo[kk][t] = LD128(oo + t * 2048 + kk * 64);
#pragma unroll
      for (int kk = 0; kk < 4; ++kk)
#pragma unroll
        for (int t = 0; t < 2; ++t) mx = umax(mx, umax4(bo[kk][t]));
      if (__builtin_amdgcn_ballot_w64(mx == SENT) == 0) break;
      CFENCE();
    }
    STAMP(9);
#pragma unroll
    for (int mt = 0; mt < 4; ++mt) {
#pragma unroll
      for (int kk = 0; kk < 4; ++kk)
#pragma unroll
        for (int t = 0; t < 2; ++t) acc[mt] = mfma(aw[kk][mt], bo[kk][t], acc[mt]);
      u32x4 o;
#pragma unroll
      for (int i = 0; i < 4; ++i) o[i] = nosent(asu(acc[mt][i] + dpp<D_R8>(acc[mt][i])));
      ST128(o, n < 8 ? pbase + ((4 * mt + g) * 8 + n) * 16 : OOB);
    }
  }
  STAMP(10);
  {
    const int rr = hk * 4 + j;
    const unsigned po = WS_P + (cg * 32 + (lane >> 2)) * 2048 + (rr * 4 + (lane & 3)) * 16;
    u32x4 a, c;
#pragma nounroll
    for (int it = 0; it < SPIN_MEM; ++it) {
      a = LD128(po); c = LD128(po + 16 * 2048);
      if (__builtin_amdgcn_ballot_w64(umax(umax4(a), umax4(c)) == SENT) == 0) break;
      CFENCE();
    }
    STAMP(11);
    float o[4];
#pragma unroll
    for (int i = 0; i < 4; ++i) { float t = asf(a[i]) + asf(c[i]); t += dpp<D_R4>(t); t += dpp<D_R8>(t); o[i] = xrow_sum(t); }
    if (lane < 4) *(u32x2*)(out + (4 * (rr & 1) + lane) * 448 + 64 * cg + 4 * (rr >> 1)) = u32x2{pk(o[0], o[1]), pk(o[2], o[3])};
  }
  STAMP(12);
  STAMP_OUT();
}

extern "C" void kernel_launch(void* const* d_in, const int* in_sizes, int n_in, void* d_out, int out_size, void* d_ws, size_t ws_size,
                              hipStream_t stream) {
  gated_mix_kernel<<<256, 320, 0, stream>>>((const unsigned short*)d_in[0], (const unsigned short*)d_in[1], (const unsigned short*)d_in[2],
                                            (const unsigned short*)d_in[3], (const unsigned short*)d_in[4], (const unsigned short*)d_in[6],
                                            (const unsigned short*)d_in[7], (const unsigned short*)d_in[8], (unsigned short*)d_out,
                                            (unsigned char*)d_ws);
}
```

```cpp
#include <hip/hip_runtime.h>
#include <stdint.h>

#define LAS __attribute__((address_space(3)))
typedef unsigned u32x4 __attribute__((ext_vector_type(4)));
typedef unsigned u32x2 __attribute__((ext_vector_type(2)));
typedef float f32x4 __attribute__((ext_vector_type(4)));
typedef float f32x2 __attribute__((ext_vector_type(2)));
typedef __bf16 bf16x8 __attribute__((ext_vector_type(8)));
typedef __bf16 bf16x2 __attribute__((ext_vector_type(2)));
typedef short s16x4 __attribute__((ext_vector_type(4)));
typedef unsigned long long u64;
typedef u64 u64x2 __attribute__((ext_vector_type(2)));
typedef u64 u64x8 __attribute__((ext_vector_type(8)));

#define TAU 56
#define OFF 30
#define BAND 30
#define PRQ 15
#define PRS 46
#define DLY1 10
#define DLY2 16
#define CODEB 13824

constexpr int SK = 8192, WIN = 2048;
constexpr unsigned WS_E = 0, WS_ORG = 1024, WS_FLAG = 4096, WS_ST = 8192, WS_H = 65536, WS_Q = WS_H + 262144, WS_O = WS_Q + 262144, WS_P = WS_O + 262144,
                   WS_END = WS_P + 224 * 2048;
constexpr unsigned OOB = 0x7ffffff0u;
constexpr unsigned SENT = 0xffffffffu;
constexpr int SC1 = 16, SC1NT = 18;
constexpr unsigned L_X = 0, L_G = 7168, L_C = 8192, L_Q = 16384, L_F = 17408, L_U = 18432, L_D = L_U, L_H = L_U + 13312, L_R = 32768, L_END = 163840;
constexpr unsigned F_Q = L_F, F_F = L_F + 4, F_LD = L_F + 8, F_ORG = L_F + 12, F_TAG = L_F + 16, F_ST = L_F + 20, F_AR = L_F + 24, F_FC = L_F + 32, F_W = L_F + 48,
                   L_STL = L_F + 64, L_FF = L_F + 128, L_DUM = L_F + 256, L_TR = L_F + 512;
constexpr int SPIN_LDS = 1 << 20, SPIN_MEM = 1 << 16;
constexpr float SCL = 0.0625f * 1.4426950408889634f;

#define WAITVM0() asm volatile("s_waitcnt vmcnt(0)" ::: "memory")
#define WAITLGKM0() asm volatile("s_waitcnt lgkmcnt(0)" ::: "memory")
#define CFENCE() asm volatile("" ::: "memory")
#define LD128(off, pol) __builtin_amdgcn_raw_buffer_load_b128(rws, (off), 0, (pol))
#define ST128(v, off, pol) __builtin_amdgcn_raw_buffer_store_b128((v), rws, (off), 0, (pol))
#define ST64(v, off) __builtin_amdgcn_raw_buffer_store_b64((v), rws, (off), 0, SC1)

__device__ __forceinline__ u32x4 make_srd(u64 a, unsigned bytes) { return u32x4{(unsigned)a, (unsigned)(a >> 32) & 0xffffu, bytes, 0x00020000u}; }
__device__ __forceinline__ void dma_nt(unsigned ldsaddr, unsigned voff, u32x4 srd, unsigned soff) {
  asm volatile("s_mov_b32 m0, %0\n\ts_nop 0\n\tbuffer_load_dwordx4 %1, %2, %3 offen nt lds" ::"s"(ldsaddr), "v"(voff), "s"(srd), "s"(soff) : "memory");
}
__device__ __forceinline__ void dma_sh(unsigned ldsaddr, unsigned voff, u32x4 srd, unsigned soff) {
  asm volatile("s_mov_b32 m0, %0\n\ts_nop 0\n\tbuffer_load_dwordx4 %1, %2, %3 offen lds" ::"s"(ldsaddr), "v"(voff), "s"(srd), "s"(soff) : "memory");
}
__device__ __forceinline__ f32x4 mfma32(u32x4 a, u32x4 b, f32x4 c) {
  return __builtin_amdgcn_mfma_f32_16x16x32_bf16(__builtin_bit_cast(bf16x8, a), __builtin_bit_cast(bf16x8, b), c, 0, 0, 0);
}
__device__ __forceinline__ f32x4 mfma16(u32x2 a, u32x2 b, f32x4 c) {
  return __builtin_amdgcn_mfma_f32_16x16x16bf16_1k(__builtin_bit_cast(s16x4, a), __builtin_bit_cast(s16x4, b), c, 0, 0, 0);
}
__device__ __forceinline__ u32x2 trread(LAS unsigned char* p) { return __builtin_bit_cast(u32x2, __builtin_amdgcn_ds_read_tr16_b64_v4i16((LAS s16x4*)p)); }
__device__ __forceinline__ u32x4 trfrag(LAS unsigned char* p0, LAS unsigned char* p1) {
  const u32x2 a = trread(p0), b = trread(p1);
  return u32x4{a[0], a[1], b[0], b[1]};
}
__device__ __forceinline__ unsigned pk(float lo, float hi) { const bf16x2 v = {(__bf16)lo, (__bf16)hi}; return __builtin_bit_cast(unsigned, v); }
__device__ __forceinline__ float asf(unsigned u) { return __builtin_bit_cast(float, u); }
__device__ __forceinline__ unsigned asu(float f) { return __builtin_bit_cast(unsigned, f); }
__device__ __forceinline__ float bflo(unsigned u) { return asf(u << 16); }
__device__ __forceinline__ float bfhi(unsigned u) { return asf(u & 0xffff0000u); }
__device__ __forceinline__ unsigned umin(unsigned a, unsigned b) { return a < b ? a : b; }
__device__ __forceinline__ unsigned umax(unsigned a, unsigned b) { return a > b ? a : b; }
__device__ __forceinline__ unsigned umax4(u32x4 v) { return umax(umax(v[0], v[1]), umax(v[2], v[3])); }
__device__ __forceinline__ unsigned nosent(unsigned u) { return umin(u, SENT - 1u); }
template <int CTRL> __device__ __forceinline__ float dpp(float x) { return asf(__builtin_amdgcn_mov_dpp(asu(x), CTRL, 0xf, 0xf, true)); }
constexpr int D_X1 = 0xB1, D_X2 = 0x4E, D_R4 = 0x124, D_R8 = 0x128;
__device__ __forceinline__ float xrow_max(float x) {
  auto s = __builtin_amdgcn_permlane16_swap(asu(x), asu(x), false, false); x = fmaxf(asf(s[0]), asf(s[1]));
  auto t = __builtin_amdgcn_permlane32_swap(asu(x), asu(x), false, false); return fmaxf(asf(t[0]), asf(t[1]));
}
__device__ __forceinline__ float xrow_sum(float x) {
  auto s = __builtin_amdgcn_permlane16_swap(asu(x), asu(x), false, false); x = asf(s[0]) + asf(s[1]);
  auto t = __builtin_amdgcn_permlane32_swap(asu(x), asu(x), false, false); return asf(t[0]) + asf(t[1]);
}
template <int SLP> __device__ __forceinline__ void lds_wait_ge(LAS unsigned char* L, unsigned off, unsigned want) {
#pragma nounroll
  for (int i = 0; i < SPIN_LDS; ++i) {
    if ((unsigned)__builtin_amdgcn_readfirstlane(*(volatile LAS unsigned*)(L + off)) >= want) break;
    if (SLP) __builtin_amdgcn_s_sleep(SLP);
  }
  CFENCE();
}
__device__ __forceinline__ void lds_wait4_ge(LAS unsigned char* L, unsigned off, unsigned want) {
#pragma nounroll
  for (int i = 0; i < SPIN_LDS; ++i) {
    const u32x4 v = *(volatile LAS u32x4*)(L + off);
    if ((unsigned)__builtin_amdgcn_readfirstlane(umin(umin(v[0], v[1]), umin(v[2], v[3]))) >= want) break;
    __builtin_amdgcn_s_sleep(1);
  }
  CFENCE();
}

#define ROW4(N, V) "s_waitcnt vmcnt(" #N ")\n\tbuffer_load_dwordx4 " V ", s[60:63], s64 offen nt lds\n\t"              \
                   "s_waitcnt vmcnt(" #N ")\n\tbuffer_load_dwordx4 " V ", s[60:63], s65 offen offset:1024 nt lds\n\t"  \
                   "s_waitcnt vmcnt(" #N ")\n\tbuffer_load_dwordx4 " V ", s[60:63], s66 offen offset:2048 nt lds\n\t"  \
                   "s_waitcnt vmcnt(" #N ")\n\tbuffer_load_dwordx4 " V ", s[60:63], s67 offen offset:3072 nt lds\n\t"  \
                   "s_add_u32 m0, m0, 4096\n\t"
#define PUBLISH "ds_write_b32 v45, v47 offset:8\n\t"
#define STEP_(N, D) "v_add_u32 v47, " #D ", v46\n\t" ROW4(N, "%[v0]") ROW4(N, "%[v1]") PUBLISH "v_add_u32 v47, 1, v47\n\t" \
                    ROW4(N, "%[v2]") ROW4(N, "%[v3]") PUBLISH "s_branch .Lnext%=\n"
#define STEP(...) STEP_(__VA_ARGS__)
#define LV0 29, -3
#define LV1 25, -3
#define LV2 22, -2
#define LV3 19, -2
#define LV4 17, -2
#define LV5 15, -1


__global__ void __launch_bounds__(384, 2)
gated_mix_kernel(const unsigned short*, const unsigned short*, const unsigned short*, const unsigned short*, unsigned char*, const unsigned short*,
                 const unsigned short*, const unsigned short*, const unsigned short*, unsigned short*) {
  __shared__ __attribute__((aligned(16))) unsigned char smem[L_END];
  LAS unsigned char* L = (LAS unsigned char*)smem;
  const unsigned lds0 = (unsigned)(size_t)L;
  asm volatile("s_load_dword s98, %0, 0x0\n\ts_load_dword s99, %0, 0x40" ::"s"(__builtin_amdgcn_kernarg_segment_ptr()) : "s98", "s99");
  __builtin_amdgcn_sched_barrier(0);
#define ARGS()                                                                                      \
  u64x8 ka; u64x2 kb;                                                                               \
  asm volatile("s_load_dwordx16 %0, %2, 0x0\n\ts_load_dwordx4 %1, %2, 0x40\n\ts_waitcnt lgkmcnt(0)" \
               : "=&s"(ka), "=&s"(kb) : "s"(__builtin_amdgcn_kernarg_segment_ptr()) : "memory")
  const int lane = threadIdx.x & 63, wave = __builtin_amdgcn_readfirstlane(threadIdx.x >> 6);
  const int wg = blockIdx.x, hk = wg & 7, r = wg >> 3, b = r >> 2, s = r & 3;
  const int g = lane >> 4, n = lane & 15;
  const unsigned vrow = lane >> 3, vcol = ((lane & 7) ^ (vrow & 6)) * 16;
  const unsigned trw = (4 * g + (n >> 2)) * 128 + (2 * (g & 1) + (n >> 3)) * 32 + (lane & 3) * 8;
  const int j = s, cg = b, hc = (hk + 1) & 7;
  const bool ffn = hk < 7 && r < 28;
  if (wave == 5) *(LAS u32x4*)(L + L_F + lane * 16) = u32x4{0u, 0u, 0u, 0u};
  if (wave == 4) {
    u64 pc;
    asm volatile("s_getpc_b64 %0" : "=s"(pc));
    asm volatile("s_mov_b32 m0, %0\n\ts_nop 0\n\tbuffer_load_dword %1, %2, 0 offen lds" ::"s"(lds0 + L_DUM), "v"(((r & 7) + 8 * lane) * 128), "s"(make_srd(pc & ~255ull, CODEB)) : "memory");
  }
  if (wave < 4) {
    ARGS();
    const u32x4 rq = make_srd(ka[1], 448 * 8192), rx = make_srd(ka[2], 7168), r1 = make_srd(ka[5], 448 * 3584);
    const unsigned so = (112 * s + 8 * wave) * 8192 + hk * 1024 + b * 128, vq = vrow * 8192 + vcol;
    dma_nt(lds0 + L_U + wave * 1024, vq, rq, so);
    dma_nt(lds0 + L_U + 4096 + wave * 1024, vq, rq, so + 32 * 8192);
    dma_nt(lds0 + L_U + 8192 + wave * 1024, vq, rq, so + 64 * 8192);
    if (wave < 2) dma_nt(lds0 + L_U + 12288 + wave * 1024, vq, rq, so + 96 * 8192);
    else if (wave == 2) dma_sh(lds0 + L_Q, lane * 16, make_srd(ka[4] + WS_E, 16), 0);
    dma_sh(lds0 + L_X + wave * 1024, lane * 16, rx, wave * 1024);
    if (wave < 3) dma_sh(lds0 + L_X + 4096 + wave * 1024, lane * 16, rx, 4096 + wave * 1024);
    else dma_sh(lds0 + L_G, lane * 16, make_srd(ka[3], 896), 0);
    const unsigned v1w = ffn ? vrow * 3584 + vcol : OOB, s1 = (64 * b + 8 * wave) * 3584 + (hk * 4 + s) * 128;
    dma_nt(lds0 + L_C + wave * 1024, v1w, r1, s1);
    dma_nt(lds0 + L_C + 4096 + wave * 1024, v1w, r1, s1 + 32 * 3584);
  }
  CFENCE();
  if (wave == 5) {
    __builtin_amdgcn_s_setprio(3);
    const unsigned hi = lane >> 5, va = hi * 512 + (((lane & 31) ^ (hi << 1)) << 4);
    const unsigned vw = vrow * 896 + vcol;
    asm volatile("s_load_dwordx2 s[60:61], %[ka], 0x0\n\ts_load_dwordx4 s[76:79], %[ka], 0x30\n\ts_load_dwordx2 s[80:81], %[ka], 0x40\n\ts_memrealtime s[72:73]\n\t"
                 "s_mov_b32 s62, 2048*512\n\ts_mov_b32 s63, 0x20000\n\t"
                 "s_mov_b32 s64, %[so]\n\ts_add_u32 s65, %[so], 8192-1024\n\ts_add_u32 s66, %[so], 2*8192-2048\n\ts_add_u32 s67, %[so], 3*8192-3072\n\t"
                 "s_mov_b32 m0, %[ring]\n\ts_mov_b32 s68, 0\n\ts_movk_i32 s69, 4096\n\ts_mov_b32 s70, %[due]\n\t"
                 "v_mov_b32 v40, 0\n\tv_mov_b32 v41, 0\n\tv_mov_b32 v42, 0\n\tv_mov_b32 v43, 0\n\tv_mov_b32 v44, 0\n\t"
                 "v_mov_b32 v45, %[ctl]\n\tv_mov_b32 v46, 16\n\tv_mov_b32 v49, %[ctl]\n\t"
                 "s_waitcnt lgkmcnt(0)\n\t"
                 "s_add_u32 s60, s60, %[win]\n\ts_addc_u32 s61, s61, 0\n\ts_and_b32 s61, s61, 0xffff\n\ts_branch .Lfirst%=\n"
                 ".Lnext%=:\n\t"
                 "s_add_u32 s64, s64, s69\n\ts_add_u32 s65, s65, s69\n\ts_add_u32 s66, s66, s69\n\ts_add_u32 s67, s67, s69\n\ts_xor_b32 s69, s69, 0x1e000\n\t"
                 "v_add_u32 v46, 2, v46\n\ts_add_u32 s68, s68, 1\n\ts_and_b32 s74, s68, 6\n\ts_cbranch_scc0 .Lrare%=\n"
                 ".Lloop%=:\n\t"
                 "s_waitcnt lgkmcnt(0)\n\t"
                 "v_min_u32 v48, v40, v41\n\tv_min3_u32 v48, v48, v42, v43\n\tv_readfirstlane_b32 s71, v44\n\tv_readfirstlane_b32 s74, v48\n\t"
                 "v_mov_b32 v48, s72\n\ts_lshr_b32 s75, s68, 1\n\ts_add_u32 s74, s74, 3\n\ts_cmp_lt_u32 s74, s75\n\ts_cbranch_scc1 .Lfull%=\n"
                 ".Lgo%=:\n\t"
                 "s_cmp_eq_u32 s68, 32\n\ts_cbranch_scc1 .Ltail%=\n\t"
                 "ds_write_b32 v49, v48 offset:516\n\tv_add_u32 v49, 4, v49\n\t"
                 "s_add_u32 s70, s70, %[tau]\n\ts_add_u32 s74, s71, s70\n\ts_sub_u32 s74, s74, s72\n\ts_cmp_eq_u32 s71, 0\n\ts_cselect_b32 s74, -1, s74\n\t"
                 "s_memrealtime s[72:73]\n\tds_read_b128 v[40:43], v45 offset:32\n\tds_read_b32 v44, v45 offset:12\n\t"
                 "s_cmp_ge_i32 s74, 0\n\ts_cbranch_scc1 .Lahead%=\n\t"
                 "s_cmp_lt_i32 s74, -%[band]\n\ts_cbranch_scc1 .Llate%=\n"
                 ".Lfirst%=:\n\t"
                 STEP(LV2)
                 ".Lrare%=:\n\t"
                 "s_bitcmp1_b32 s68, 0\n\ts_cbranch_scc0 .Lwrap%=\n\ts_cmp_lg_u32 s68, 1\n\ts_cbranch_scc1 .Lloop%=\n\t"
                 "s_add_u32 s76, s76, %[win]\n\ts_addc_u32 s77, s77, 0\n\ts_and_b32 s77, s77, 0xffff\n\ts_and_b32 s79, s79, 0xffff\n\ts_and_b32 s81, s81, 0xffff\n\t"
                 "s_waitcnt lgkmcnt(0)\n\ts_barrier\n\ts_branch .Lloop%=\n"
                 ".Lwrap%=:\n\ts_mov_b32 m0, %[ring]\n\ts_cmp_lg_u32 s68, 16\n\ts_cbranch_scc1 .Lloop%=\n\t"
                 "s_mov_b64 s[60:61], s[76:77]\n\t"
                 "s_mov_b32 s64, %[so]\n\ts_add_u32 s65, %[so], 8192-1024\n\ts_add_u32 s66, %[so], 2*8192-2048\n\ts_add_u32 s67, %[so], 3*8192-3072\n\ts_branch .Lloop%=\n"
                 ".Lahead%=:\n\ts_cmp_ge_i32 s74, %[band]\n\ts_cbranch_scc1 .Learly%=\n\t"
                 STEP(LV3)
                 ".Ltail%=:\n\t"
                 "s_mov_b64 s[60:61], s[80:81]\n\ts_mov_b32 s62, 1792*896\n\t"
                 "s_mov_b32 s64, %[s2]\n\ts_add_u32 s65, %[s2], 16*896-1024\n\ts_add_u32 s66, %[s2], 32*896-2048\n\ts_add_u32 s67, %[s2], 48*896-3072\n\t"
                 "v_mov_b32 v47, 16+61\n\tv_add_u32 v40, 8*896, %[vo]\n\tv_add_u32 v41, 32*896, %[vo]\n\tv_add_u32 v42, 40*896, %[vo]\n\tv_add_u32 v43, 8*896, %[v2w]\n\t"
                 "s_waitcnt vmcnt(24)\n\t" PUBLISH "v_mov_b32 v47, 16+62\n\t"
                 "s_waitcnt vmcnt(16)\n\t" PUBLISH "v_mov_b32 v47, 16+63\n\t"
                 ROW4(23, "%[v2w]") "s_waitcnt vmcnt(12)\n\t" PUBLISH "v_mov_b32 v47, 16+64\n\t"
                 ROW4(23, "v43")
                 "s_mov_b64 s[60:61], s[78:79]\n\ts_mov_b32 s62, 4096*896\n\t"
                 "s_mov_b32 s64, %[sw]\n\ts_add_u32 s65, %[sw], 16*896-1024\n\ts_add_u32 s66, %[sw], 64*896-2048\n\ts_add_u32 s67, %[sw], 80*896-3072\n\t"
                 ROW4(23, "%[vo]") "s_waitcnt vmcnt(12)\n\t" PUBLISH "v_mov_b32 v47, 16+65\n\t"
                 ROW4(23, "v40") ROW4(23, "v41") ROW4(23, "v42")
                 "s_waitcnt vmcnt(16)\n\t" PUBLISH "v_mov_b32 v47, 16+67\n\t"
                 "s_waitcnt vmcnt(0)\n\t" PUBLISH "s_branch .Lend%=\n"
                 ".Llate%=:\n\ts_cmp_lt_i32 s74, -2*%[band]\n\ts_cbranch_scc1 .Llate2%=\n\t"
                 STEP(LV1)
                 ".Llate2%=:\n\t"
                 STEP(LV0)
                 ".Learly%=:\n\ts_cmp_ge_i32 s74, 2*%[band]\n\ts_cbranch_scc1 .Learly2%=\n\t"
                 STEP(LV4)
                 ".Learly2%=:\n\t"
                 STEP(LV5)
                 ".Lfull%=:\n\ts_mov_b32 s73, 0x100000\n"
                 ".Lfull1%=:\n\ts_sleep 1\n\tds_read_b128 v[40:43], v45 offset:32\n\ts_waitcnt lgkmcnt(0)\n\ts_sub_u32 s73, s73, 1\n\ts_cbranch_scc1 .Lgo%=\n\t"
                 "v_min_u32 v47, v40, v41\n\tv_min3_u32 v47, v47, v42, v43\n\ts_nop 0\n\tv_readfirstlane_b32 s74, v47\n\ts_nop 3\n\t"
                 "s_add_u32 s74, s74, 3\n\ts_cmp_lt_u32 s74, s75\n\ts_cbranch_scc1 .Lfull1%=\n\ts_branch .Lgo%=\n"
                 ".Lend%=:\n\ts_waitcnt lgkmcnt(0)"
                 ::[v0] "v"(va), [v1] "v"((va ^ 64) + 1024), [v2] "v"((va ^ 128) + 2048), [v3] "v"((va ^ 192) + 3072),
                   [vo] "v"(r < 28 ? vw : OOB), [v2w] "v"(ffn ? vw : OOB), [ka] "s"(__builtin_amdgcn_kernarg_segment_ptr()),
                   [win] "s"(((b * 8 + hk) * SK + (SK - WIN)) * 512), [so] "s"(s * 32768), [ring] "s"(lds0 + L_R), [ctl] "s"(lds0 + L_F),
                   [sw] "s"((512 * hc + 128 * j) * 896 + cg * 128), [s2] "s"(64 * (hk * 4 + j) * 896 + cg * 128),
                   [tau] "i"(TAU), [band] "i"(BAND), [due] "i"(OFF - 5 * TAU)
                 : "s60", "s61", "s62", "s63", "s64", "s65", "s66", "s67", "s68", "s69", "s70", "s71", "s72", "s73", "s74", "s75", "s76", "s77", "s78", "s79",
                   "s80", "s81", "v40", "v41", "v42", "v43", "v44", "v45", "v46", "v47", "v48", "v49", "scc", "memory");
  }
  CFENCE();
  if (wave == 5) return;
  __builtin_amdgcn_s_barrier();
  ARGS();
  const __amdgpu_buffer_rsrc_t rws = __builtin_amdgcn_make_buffer_rsrc((void*)ka[4], 0, (int)WS_END, 0x00020000);
  if (wave < 4) {
    WAITVM0();
    *(volatile LAS unsigned*)(L + F_W + wave * 4) = 1u;
    const unsigned ka0 = L_R + (n >> 1) * 4096 + wave * 1024 + (n & 1) * 512 + ((g ^ ((n & 7) << 1)) << 4);
    u32x4 kf[4][8];
#pragma unroll
    for (int c = 0; c < 4; ++c) {
      lds_wait_ge<4>(L, F_LD, 16 + 4 * c + 4);
#pragma unroll
      for (int kk = 0; kk < 8; ++kk) kf[c][kk] = *(LAS u32x4*)(L + ((ka0 ^ ((kk & 3) * 64)) + (kk >> 2) * 256) + c * 32768);
      CFENCE();
      *(volatile LAS unsigned*)(L + F_FC + wave * 4) = c + 1;
    }
    lds_wait_ge<4>(L, F_Q, 1);
    u32x4 qf[8];
#pragma unroll
    for (int kk = 0; kk < 8; ++kk) qf[kk] = *(LAS u32x4*)(L + L_Q + (n & 1) * 512 + kk * 64 + g * 16);
    LAS unsigned char* C = L + L_C + wave * 2048;
#pragma unroll
    for (int c = 0; c < 4; ++c) {
      f32x4 sc = {0.f, 0.f, 0.f, 0.f};
#pragma unroll
      for (int kk = 0; kk < 8; ++kk) sc = mfma32(kf[c][kk], qf[kk], sc);
      if (n < 2) *(LAS f32x4*)(C + c * 128 + n * 64 + g * 16) = sc;
    }
#pragma nounroll
    for (int c = 0; c < 4; ++c) {
      lds_wait_ge<4>(L, F_LD, 16 + 4 * c + 20);
      f32x4 sc = {0.f, 0.f, 0.f, 0.f};
#pragma unroll
      for (int kk = 0; kk < 8; ++kk) sc = mfma32(*(LAS u32x4*)(L + (((ka0 + c * 32768) ^ ((kk & 3) * 64)) + (kk >> 2) * 256)), qf[kk], sc);
      if (n < 2) *(LAS f32x4*)(C + (c + 4) * 128 + n * 64 + g * 16) = sc;
      CFENCE();
      *(volatile LAS unsigned*)(L + F_FC + wave * 4) = c + 5;
    }
    {
      lds_wait4_ge(L, F_FC, 8);
      const f32x4 sc = *(LAS f32x4*)(C + lane * 16);
      float m = sc[0];
#pragma unroll
      for (int w = 0; w < 4; ++w) { const f32x4 t = *(LAS f32x4*)(L + L_C + w * 2048 + lane * 16); m = fmaxf(fmaxf(m, fmaxf(t[0], t[1])), fmaxf(t[2], t[3])); }
      m = fmaxf(m, dpp<D_X1>(m)); m = fmaxf(m, dpp<D_X2>(m)); m = fmaxf(m, dpp<D_R8>(m)); m = xrow_max(m);
      const float p0 = __builtin_amdgcn_exp2f((sc[0] - m) * SCL), p1 = __builtin_amdgcn_exp2f((sc[1] - m) * SCL),
                  p2 = __builtin_amdgcn_exp2f((sc[2] - m) * SCL), p3 = __builtin_amdgcn_exp2f((sc[3] - m) * SCL);
      float l = (p0 + p1) + (p2 + p3);
      l += dpp<D_X1>(l); l += dpp<D_X2>(l); l += dpp<D_R8>(l); l = xrow_sum(l);
      *(LAS u32x2*)(C + 1024 + lane * 8) = u32x2{pk(p0, p1), pk(p2, p3)};
      if ((lane & ~4) == 0) *(LAS f32x2*)(L + L_STL + wave * 16 + lane * 2) = f32x2{m, l};
      CFENCE();
      __hip_atomic_fetch_add((LAS unsigned*)(L + (lane ? L_DUM + lane * 4 : F_ST)), 1u, __ATOMIC_RELAXED, __HIP_MEMORY_SCOPE_WORKGROUP);
    }
    f32x4 acc0 = {0.f, 0.f, 0.f, 0.f}, acc1 = {0.f, 0.f, 0.f, 0.f};
    unsigned va = L_R + (n >> 3) * 4096 + g * 1024 + ((n >> 2) & 1) * 512 + (wave >> 1) * 256 + ((((wave & 1) << 2) ^ (n >> 2)) << 5) + (lane & 3) * 8;
    const bool phi = (n & 2) != 0;
    float fsc = 0.f;
    unsigned opub = n < 4 ? WS_O + (hk * 32 + r) * 1024 + (2 * (n & 1) + (wave >> 1)) * 256 + (2 * (wave & 1) + (n >> 1)) * 64 + g * 16 : OOB;
    asm volatile("" : "+v"(opub));
    lds_wait_ge<1>(L, F_ST, 4);
#pragma nounroll
    for (int u = 0; u < 32; ++u) {
      const u32x2 p = *(LAS u32x2*)(L + L_C + g * 2048 + 1024 + (u >> 2) * 64 + (n & 1) * 32 + (u & 3) * 8);
      u32x4 pf = {phi ? 0u : p[0], phi ? 0u : p[1], phi ? p[0] : 0u, phi ? p[1] : 0u};
      unsigned a0 = va + (u & 15) * 8192, a1 = a0 ^ 32, a2 = a0 ^ 64, a3 = a0 ^ 96;
      asm volatile("" : "+v"(pf), "+v"(a0), "+v"(a1), "+v"(a2), "+v"(a3));
      if (u == 28) { lds_wait_ge<2>(L, F_F, 1); fsc = *(LAS float*)(L + L_FF + (n & 1) * 4); }
      u32x4 f0, f1;
#pragma nounroll
      for (int i = 0; i < SPIN_LDS; ++i) {
        const unsigned ld = *(volatile LAS unsigned*)(L + F_LD);
        CFENCE();
        f0 = trfrag(L + a0, L + a1); f1 = trfrag(L + a2, L + a3);
        CFENCE();
        if ((unsigned)__builtin_amdgcn_readfirstlane(ld) > (unsigned)(16 + 32 + u)) break;
        if (u < 29) __builtin_amdgcn_s_sleep(2);
      }
      acc0 = mfma32(f0, pf, acc0);
      acc1 = mfma32(f1, pf, acc1);
      if ((u & 3) == 3) *(volatile LAS unsigned*)(L + F_FC + wave * 4) = 9 + (u >> 2);
      va ^= 128;
    }
    acc0 *= fsc; acc1 *= fsc;
    ST128((u32x4{nosent(pk(acc0[0], acc0[1])), nosent(pk(acc0[2], acc0[3])), nosent(pk(acc1[0], acc1[1])), nosent(pk(acc1[2], acc1[3]))}), opub, SC1);
    if (r >= 28) return;
    unsigned ppub = WS_P + (cg * 32 + hk * 4 + j) * 2048 + ((4 * (g >> 1) + (n >> 2)) * 8 + (g & 1) * 4 + (n & 3)) * 16;
    unsigned tag = *(LAS unsigned*)(L + F_TAG);
    const unsigned oo = WS_O + (hc * 32 + 4 * (n & 7) + (n >> 3)) * 1024 + j * 256 + wave * 64 + g * 16;
    u32x2 hf = *(LAS u32x2*)(L + L_H + (n & 7) * 128 + wave * 32 + g * 8);
    if (n >= 8 || !ffn) hf = u32x2{0u, 0u};
    unsigned tw[4], arr = lane ? L_DUM + lane * 4 : F_AR;
#pragma unroll
    for (int mt = 0; mt < 4; ++mt) tw[mt] = (L_R + wave * 1024 + (g >> 1) * 4096 + (trw & 1023)) ^ (32 * mt);
    asm volatile("" : "+v"(ppub), "+v"(tag), "+v"(hf), "+v"(tw[0]), "+v"(tw[1]), "+v"(tw[2]), "+v"(tw[3]), "+v"(arr));
    __builtin_amdgcn_s_sleep(DLY1);
    u32x4 b0 = LD128(oo, SC1), b1 = LD128(oo + 2048, SC1);
    f32x4 acc[4];
    lds_wait_ge<0>(L, F_LD, 16 + 65);
#pragma unroll
    for (int mt = 0; mt < 4; ++mt) acc[mt] = mfma16(hf, trread(L + tw[mt]), f32x4{0.f, 0.f, 0.f, 0.f});
    asm volatile("" : "+v"(acc[0]), "+v"(acc[1]), "+v"(acc[2]), "+v"(acc[3]));
    lds_wait_ge<0>(L, F_LD, 16 + 67);
    u32x4 aw[4];
#pragma unroll
    for (int mt = 0; mt < 4; ++mt) aw[mt] = trfrag(L + 8192 + tw[mt], L + 16384 + tw[mt]);
#pragma nounroll
    for (int it = 0; it < SPIN_MEM && umax(umax4(b0), umax4(b1)) == SENT; ++it) {
      CFENCE();
      b0 = LD128(oo, SC1); b1 = LD128(oo + 2048, SC1);
    }
#pragma unroll
    for (int mt = 0; mt < 4; ++mt) acc[mt] = mfma32(b1, aw[mt], mfma32(b0, aw[mt], acc[mt]));
    f32x4 q0, q1;
#pragma unroll
    for (int i = 0; i < 4; ++i) {
      auto x0 = __builtin_amdgcn_permlane32_swap(asu(acc[0][i]), asu(acc[1][i]), false, false); q0[i] = asf(x0[0]) + asf(x0[1]);
      auto x1 = __builtin_amdgcn_permlane32_swap(asu(acc[2][i]), asu(acc[3][i]), false, false); q1[i] = asf(x1[0]) + asf(x1[1]);
    }
    *(LAS f32x4*)(L + L_D + wave * 2048 + lane * 16) = q0; *(LAS f32x4*)(L + L_D + wave * 2048 + 1024 + lane * 16) = q1;
    CFENCE();
    const unsigned old = __hip_atomic_fetch_add((LAS unsigned*)(L + arr), 1u, __ATOMIC_RELAXED, __HIP_MEMORY_SCOPE_WORKGROUP);
    CFENCE();
    f32x4 u[8];
#pragma unroll
    for (int i = 0; i < 8; ++i) u[i] = *(LAS f32x4*)(L + L_D + lane * 16 + i * 1024);
    CFENCE();
    const f32x4 o0 = (u[0] + u[2]) + (u[4] + u[6]), o1 = (u[1] + u[3]) + (u[5] + u[7]);
    const unsigned po = __builtin_amdgcn_readfirstlane(old) == 3 ? ppub : OOB;
    ST128((u32x4{pk(o0[0], o0[1]), tag, pk(o0[2], o0[3]), tag}), po, SC1);
    ST128((u32x4{pk(o1[0], o1[1]), tag, pk(o1[2], o1[3]), tag}), po + 1024, SC1);
    return;
  }

  lds_wait4_ge(L, F_W, 1);
  const u32x2 ep = *(LAS u32x2*)(L + L_Q);
  const unsigned t0 = __builtin_amdgcn_readfirstlane(ep[0]) + 1u;
  const unsigned t1 = __builtin_amdgcn_readfirstlane(ep[1]) + (t0 == 0u ? 1u : 0u);
  *(LAS unsigned*)(L + F_TAG) = t0;
  float gate;
  {
    f32x4 d[4], e[4];
#pragma unroll
    for (int mt = 0; mt < 4; ++mt) d[mt] = e[mt] = f32x4{0.f, 0.f, 0.f, 0.f};
#pragma unroll
    for (int ks = 0; ks < 7; ++ks) {
      const u32x2 xf = *(LAS u32x2*)(L + L_X + (n & 7) * 896 + (112 * s + 16 * ks + 4 * g) * 2);
#pragma unroll
      for (int mt = 0; mt < 4; ++mt) d[mt] = mfma16(trread(L + L_U + (trw ^ (32 * mt)) + ks * 2048), xf, d[mt]);
    }
#pragma unroll
    for (int ks = 0; ks < 4; ++ks) {
      const u32x2 xf = *(LAS u32x2*)(L + L_X + (n & 7) * 896 + (64 * b + 16 * ks + 4 * g) * 2);
#pragma unroll
      for (int mt = 0; mt < 4; ++mt) e[mt] = mfma16(trread(L + L_C + (trw ^ (32 * mt)) + ks * 2048), xf, e[mt]);
    }
    const f32x4 ql = n < 8 ? d[0] : d[2], qh = n < 8 ? d[1] : d[3], hl = n < 8 ? e[0] : e[2], hh = n < 8 ? e[1] : e[3];
    u32x4 qv = {pk(ql[0], ql[1]), pk(ql[2], ql[3]), pk(qh[0], qh[1]), pk(qh[2], qh[3])};
    u32x4 hv = {pk(hl[0], hl[1]), pk(hl[2], hl[3]), pk(hh[0], hh[1]), pk(hh[2], hh[3])};
    asm volatile("" : "+v"(qv), "+v"(hv)::"memory");
    const unsigned sl = ((n >> 3) * 4 + g) * 16;
    ST128(qv, WS_Q + (((n & 7) * 8 + hk) * 4 + s) * 1024 + b * 128 + sl, SC1NT);
    ST128(hv, ffn ? WS_H + ((hk * 4 + s) * 8 + b) * 1024 + (n & 7) * 128 + sl : OOB, SC1NT);
    ST128((u32x4{SENT, SENT, SENT, SENT}), WS_O + (hk * 32 + r) * 1024 + lane * 16, SC1NT);
    f32x4 dg = {0.f, 0.f, 0.f, 0.f};
#pragma unroll
    for (int kk = 0; kk < 14; ++kk) dg = mfma32(*(LAS u32x4*)(L + L_G + kk * 64 + g * 16), *(LAS u32x4*)(L + L_X + (n & 7) * 896 + kk * 64 + g * 16), dg);
    gate = __builtin_amdgcn_rcpf(1.f + __expf(-dg[0]));
    WAITVM0();
    ST64((u32x2{t0, t1}), lane == 0 ? WS_FLAG + (hk * 32 + r) * 8 : OOB);
  }
  lds_wait_ge<2>(L, L_TR + 20, 1);
  const unsigned t4 = *(LAS unsigned*)(L + L_TR + 20);
  ST64((u32x2{t4, t0}), ((r & 7) == 0 && lane == 0) ? WS_ORG + hk * 128 + r : OOB);
#define TOUCH(p, off) asm volatile("s_load_dword s97, %0, %1" ::"s"(p), "s"(off) : "s97")
  if (s == 0) TOUCH(ka[6], ((b * 8 + hk) * SK + (SK - WIN)) * 512);
  if (cg == 1) { TOUCH(ka[7], (512 * hc + 128 * j) * 896); TOUCH(kb[0], 56 * (hk * 4 + j) * 896); }
  if (cg == 2) TOUCH(kb[1], (r & 1) * 7040);
  WAITLGKM0();
  const unsigned lo = (lane >> 3) * 128 + ((lane >> 2) & 1) * 64 + (lane & 3) * 8;
  lds_wait_ge<4>(L, F_LD, 16 + PRQ);
  u64 miss = 0;
  {
    u32x2 org;
#pragma nounroll
    for (int it = 0; it < SPIN_MEM; ++it) {
      const u32x4 a = LD128(WS_FLAG + (g ? hc : hk) * 256 + n * 16, SC1);
      org = __builtin_amdgcn_raw_buffer_load_b64(rws, WS_ORG + (lane & 7) * 128 + ((lane >> 3) & 3) * 8, 0, SC1);
      miss = __builtin_amdgcn_ballot_w64(((a[0] ^ t0) | (a[1] ^ t1) | (a[2] ^ t0) | (a[3] ^ t1)) != 0);
      if ((miss & 0xffff) == 0) break;
      __builtin_amdgcn_s_sleep(16);
      CFENCE();
    }
    CFENCE();
    u32x4 a[4];
#pragma unroll
    for (int i = 0; i < 4; ++i) a[i] = LD128(WS_Q + (b * 8 + hk) * 4096 + i * 1024 + lane * 16, SC1NT);
    CFENCE();
    {
      const int dl = (int)(org[0] - t4);
      const bool ok = org[1] == t0 && dl > -400 && dl < 400;
      float sm = ok ? (float)dl : 0.f;
      sm += dpp<D_X1>(sm); sm += dpp<D_X2>(sm); sm += dpp<D_R4>(sm); sm += dpp<D_R8>(sm); sm = xrow_sum(sm);
      const int cnt = __builtin_popcountll(__builtin_amdgcn_ballot_w64(ok));
      if (cnt > 32) *(LAS unsigned*)(L + F_ORG) = (t4 + (int)(sm * __builtin_amdgcn_rcpf((float)cnt))) | 1u;
    }
    float v[8];
#pragma unroll
    for (int i = 0; i < 4; ++i) {
      v[2 * i] = (bflo(a[0][i]) + bflo(a[1][i])) + (bflo(a[2][i]) + bflo(a[3][i]));
      v[2 * i + 1] = (bfhi(a[0][i]) + bfhi(a[1][i])) + (bfhi(a[2][i]) + bfhi(a[3][i]));
    }
    *(LAS u32x2*)(L + L_Q + lo) = u32x2{pk(v[0], v[1]), pk(v[2], v[3])};
    *(LAS u32x2*)(L + L_Q + lo + 32) = u32x2{pk(v[4], v[5]), pk(v[6], v[7])};
    CFENCE();
    *(volatile LAS unsigned*)(L + F_Q) = 1u;
  }
  if (ffn) {
    u32x4 c[7];
#pragma unroll
    for (int i = 0; i < 7; ++i) c[i] = LD128(WS_H + (hk * 4 + j) * 8192 + i * 1024 + lane * 16, SC1NT);
    const float og = 1.f - __shfl(gate, lane >> 3);
    float v[8];
#pragma unroll
    for (int i = 0; i < 4; ++i) {
      v[2 * i] = ((bflo(c[0][i]) + bflo(c[1][i])) + (bflo(c[2][i]) + bflo(c[3][i]))) + ((bflo(c[4][i]) + bflo(c[5][i])) + bflo(c[6][i]));
      v[2 * i + 1] = ((bfhi(c[0][i]) + bfhi(c[1][i])) + (bfhi(c[2][i]) + bfhi(c[3][i]))) + ((bfhi(c[4][i]) + bfhi(c[5][i])) + bfhi(c[6][i]));
    }
#pragma unroll
    for (int i = 0; i < 8; ++i) v[i] = og * v[i] * __builtin_amdgcn_rcpf(1.f + __expf(-v[i]));
    *(LAS u32x2*)(L + L_H + lo) = u32x2{pk(v[0], v[1]), pk(v[2], v[3])};
    *(LAS u32x2*)(L + L_H + lo + 32) = u32x2{pk(v[4], v[5]), pk(v[6], v[7])};
  }
  {
    const unsigned stoff = WS_ST + (hk * 8 + b) * 512;
    lds_wait_ge<2>(L, F_ST, 4);
    const u32x2 own = *(LAS u32x2*)(L + L_STL + (lane & 7) * 8);
    ST128((u32x4{own[0], t0, own[1], t0}), lane < 8 ? stoff + s * 128 + lane * 16 : OOB, SC1);
#pragma nounroll
    for (int it = 0; it < SPIN_MEM && miss; ++it) {
      const u32x4 a = LD128(WS_FLAG + hc * 256 + n * 16, SC1);
      if (__builtin_amdgcn_ballot_w64(((a[0] ^ t0) | (a[1] ^ t1) | (a[2] ^ t0) | (a[3] ^ t1)) != 0) == 0) break;
      __builtin_amdgcn_s_sleep(16);
      CFENCE();
    }
    lds_wait_ge<4>(L, F_LD, 16 + PRS);
    u32x4 v;
#pragma nounroll
    for (int it = 0; it < SPIN_MEM; ++it) {
      v = LD128(stoff + n * 32 + (g & 1) * 16, SC1);
      if (__builtin_amdgcn_ballot_w64(((v[1] ^ t0) | (v[3] ^ t0)) != 0) == 0) break;
      __builtin_amdgcn_s_sleep(16);
      CFENCE();
    }
    const float gb = asf(__builtin_amdgcn_readlane(asu(gate), b));
    const float m = asf(v[0]);
    float M = fmaxf(m, dpp<D_X1>(m)); M = fmaxf(M, dpp<D_X2>(M)); M = fmaxf(M, dpp<D_R4>(M)); M = fmaxf(M, dpp<D_R8>(M));
    const float e = __builtin_amdgcn_exp2f((m - M) * SCL);
    float l = asf(v[2]) * e; l += dpp<D_X1>(l); l += dpp<D_X2>(l); l += dpp<D_R4>(l); l += dpp<D_R8>(l);
    if (n == 4 * s) *(LAS float*)(L + L_FF + (g & 1) * 4) = gb * e * __builtin_amdgcn_rcpf(l);
    CFENCE();
    *(volatile LAS unsigned*)(L + F_F) = 1u;
  }
  if (r >= 28) {
    if (wg == 255) {
#pragma nounroll
      for (int it = 0; it < SPIN_MEM; ++it) {
        const u32x4 a = LD128(WS_FLAG + lane * 32, SC1), c = LD128(WS_FLAG + lane * 32 + 16, SC1);
        if (__builtin_amdgcn_ballot_w64(((a[0] ^ t0) | (a[1] ^ t1) | (a[2] ^ t0) | (a[3] ^ t1) | (c[0] ^ t0) | (c[1] ^ t1) | (c[2] ^ t0) | (c[3] ^ t1)) != 0) == 0) break;
        __builtin_amdgcn_s_sleep(16);
        CFENCE();
      }
      CFENCE();
      ST64((u32x2{t0, t1}), lane == 0 ? WS_E : OOB);
    }
    return;
  }
  {
    const int rr = hk * 4 + j;
    const unsigned po = WS_P + (cg * 32 + (lane >> 2)) * 2048 + (rr * 4 + (lane & 3)) * 16;
    const __amdgpu_buffer_rsrc_t rout = __builtin_amdgcn_make_buffer_rsrc((void*)kb[1], 0, 7168, 0x00020000);
    unsigned op = n < 4 ? ((4 * (rr & 1) + 2 * (g & 1) + (g >> 1)) * 448 + 64 * cg + 4 * (rr >> 1) + (lane & 3)) * 2 : OOB;
    asm volatile("" : "+v"(op));
    lds_wait_ge<1>(L, F_AR, 4);
    __builtin_amdgcn_s_sleep(DLY2);
    u32x4 u0, u1;
    int it = 0;
#pragma nounroll
    do {
      CFENCE();
      u0 = LD128(po, SC1); u1 = LD128(po + 16 * 2048, SC1);
    } while ((((u0[1] ^ t0) | (u0[3] ^ t0)) | ((u1[1] ^ t0) | (u1[3] ^ t0))) != 0 && ++it < SPIN_MEM);
    auto s01 = __builtin_amdgcn_permlane32_swap(asu(bflo(u0[0]) + bflo(u1[0])), asu(bfhi(u0[0]) + bfhi(u1[0])), false, false);
    auto s23 = __builtin_amdgcn_permlane32_swap(asu(bflo(u0[2]) + bflo(u1[2])), asu(bfhi(u0[2]) + bfhi(u1[2])), false, false);
    auto sx = __builtin_amdgcn_permlane16_swap(asu(asf(s01[0]) + asf(s01[1])), asu(asf(s23[0]) + asf(s23[1])), false, false);
    float o = asf(sx[0]) + asf(sx[1]);
    o += dpp<D_R4>(o); o += dpp<D_R8>(o);
    __builtin_amdgcn_raw_buffer_store_b16((unsigned short)pk(o, o), rout, op, 0, SC1);
  }
}

extern "C" void kernel_launch(void* const* d_in, const int* in_sizes, int n_in, void* d_out, int out_size, void* d_ws, size_t ws_size,
                              hipStream_t stream) {
  gated_mix_kernel<<<256, 384, 0, stream>>>((const unsigned short*)d_in[1], (const unsigned short*)d_in[3], (const unsigned short*)d_in[0],
                                            (const unsigned short*)d_in[8], (unsigned char*)d_ws, (const unsigned short*)d_in[6],
                                            (const unsigned short*)d_in[2], (const unsigned short*)d_in[4], (const unsigned short*)d_in[7],
                                            (unsigned short*)d_out);
}
```
